```python
import torch

def _softmax(x, axis=-1):
    return torch.softmax(x, dim=axis)
def _gelu(x):
    return torch.nn.functional.gelu(x, approximate="tanh")
def _silu(x):
    return torch.nn.functional.silu(x)
def _sigmoid(x):
    return torch.sigmoid(x)
def _layernorm(x, axis=-1, eps=1e-5):
    m = x.mean(dim=axis, keepdim=True)
    v = x.var(dim=axis, keepdim=True, unbiased=False)
    return (x - m) / torch.sqrt(v + eps)
def _scatter_add(tgt, idx, src, axis=0):
    return tgt.clone().index_add_(axis, idx, src)
def _segment_sum(src, seg, n):
    out = torch.zeros((n,) + src.shape[1:], dtype=src.dtype)
    return out.index_add_(0, seg, src)
def _pad(x, pw):
    flat = tuple(v for pair in reversed(pw) for v in pair)
    return torch.nn.functional.pad(x, flat)

kernel_name = 'recurrent_scan__spectral_gfnet__dense_activation_gelu__prompt'

def setup_inputs():

    _w = (lambda a: (lambda t: t.to(torch.bfloat16) if t.is_floating_point() else t)(torch.as_tensor(a)))
    def _A_setup():
        B, S, D = 1, 2048, 640
        Df = D // 2 + 1
        return {
            "x": _w((lambda *s: torch.randn(*s, dtype=torch.bfloat16) * 0.05)(B, S, D)),
            "w_r": _w((lambda *s: torch.randn(*s, dtype=torch.bfloat16) * 3.2)(S, Df) * 0.02),
            "w_i": _w((lambda *s: torch.randn(*s, dtype=torch.bfloat16) * 3.2)(S, Df) * 0.02),
        }
    _A_in = _A_setup()

    def _B_setup():
        B, S, D, F = 1, 2048, 640, 1728
        return {
            "x": _w((lambda *s: torch.randn(*s, dtype=torch.bfloat16) * 0.05)(B, S, D)),
            "w1": _w((lambda *s: torch.randn(*s, dtype=torch.bfloat16) * 3.2)(D, F) * (D ** -0.5)),
            "b1": _w((lambda *s: torch.randn(*s, dtype=torch.bfloat16) * 3.2)(F) * 0.01),
            "w2": _w((lambda *s: torch.randn(*s, dtype=torch.bfloat16) * 0.4)(F, D) * (F ** -0.5)),
            "b2": _w((lambda *s: torch.randn(*s, dtype=torch.bfloat16) * 0.4)(D) * 0.01),
        }
    _B_in = _B_setup()

    return {
        "A_x": _A_in["x"],
        "A_w_r": _A_in["w_r"],
        "A_w_i": _A_in["w_i"],
        "B_x": _B_in["x"],
        "B_w1": _B_in["w1"],
        "B_b1": _B_in["b1"],
        "B_w2": _B_in["w2"],
        "B_b2": _B_in["b2"],
    }

def reference(A_x, A_w_r, A_w_i, B_x, B_w1, B_b1, B_w2, B_b2):
    def _A_ref(x, w_r, w_i):
        B, S, D = x.shape
        X = (lambda x, axes=(-2,-1): torch.fft.rfft2(x.float(), dim=axes))(x, axes=(-2, -1))
        W = w_r + 1j * w_i
        X = X * W
        y = (lambda x, s, axes=(-2,-1): torch.fft.irfft2(x, s=s, dim=axes).to(torch.bfloat16))(X, (S, D), axes=(-2, -1))
        return y

    def _B_ref(x, w1, b1, w2, b2):
        h = torch.einsum("bsd,df->bsf", x, w1) + b1
        h = _gelu(h)
        return torch.einsum("bsf,fd->bsd", h, w2) + b2

    x = A_x
    xn = _layernorm(x, axis=-1)
    s = _A_ref(xn, A_w_r, A_w_i)
    g = _silu(_B_ref(xn, B_w1, B_b1, B_w2, B_b2))
    return x + s * g
```

```cpp
#include <hip/hip_runtime.h>
#include <hip/hip_bf16.h>
#include <stdint.h>
typedef unsigned short u16;
#define S_ 2048
#define D_ 640
#define DF 321
#define F_ 1728
__device__ inline float bf2f(u16 v){ return __uint_as_float(((unsigned)v)<<16); }
__device__ inline u16 f2bf(float f){ unsigned u=__float_as_uint(f); return (u16)((u+0x7FFF+((u>>16)&1))>>16); }
__device__ inline float rbf(float f){ return bf2f(f2bf(f)); }

__global__ void k_ln(const u16* x, float* xn){
  int r=blockIdx.x; int t=threadIdx.x; __shared__ float red[256];
  float s=0; for(int i=t;i<D_;i+=256) s+=bf2f(x[r*D_+i]);
  red[t]=s; __syncthreads(); for(int o=128;o>0;o>>=1){ if(t<o) red[t]+=red[t+o]; __syncthreads(); }
  float mean=red[0]/D_; __syncthreads();
  s=0; for(int i=t;i<D_;i+=256){ float d=bf2f(x[r*D_+i])-mean; s+=d*d; }
  red[t]=s; __syncthreads(); for(int o=128;o>0;o>>=1){ if(t<o) red[t]+=red[t+o]; __syncthreads(); }
  float var=rbf(red[0]/D_); float m=rbf(mean);
  float den=rbf(sqrtf(rbf(var+1e-5f)));
  for(int i=t;i<D_;i+=256){ xn[r*D_+i]=rbf(rbf(bf2f(x[r*D_+i])-m)/den); }
}
__global__ void k_rowdft(const float* xn, float2* X){
  int r=blockIdx.x; __shared__ float row[D_]; __shared__ float2 tw[D_];
  for(int i=threadIdx.x;i<D_;i+=blockDim.x){ row[i]=xn[r*D_+i]; float sn,cs; sincospif(-2.0f*i/D_,&sn,&cs); tw[i]=make_float2(cs,sn);}
  __syncthreads();
  for(int k=threadIdx.x;k<DF;k+=blockDim.x){ float re=0,im=0; int idx=0; for(int d=0;d<D_;d++){ re+=row[d]*tw[idx].x; im+=row[d]*tw[idx].y; idx+=k; if(idx>=D_) idx-=D_; } X[k*S_+r]=make_float2(re,im);}
}
__global__ void k_col(float2* X, const u16* wr, const u16* wi){
  int k=blockIdx.x; __shared__ float2 a[S_]; __shared__ float2 b[S_]; __shared__ float2 tw[S_];
  for(int i=threadIdx.x;i<S_;i+=blockDim.x){ a[i]=X[k*S_+i]; float sn,cs; sincospif(-2.0f*i/S_,&sn,&cs); tw[i]=make_float2(cs,sn);}
  __syncthreads();
  for(int q=threadIdx.x;q<S_;q+=blockDim.x){ float re=0,im=0; int idx=0; for(int s=0;s<S_;s++){ float2 t=tw[idx]; re+=a[s].x*t.x-a[s].y*t.y; im+=a[s].x*t.y+a[s].y*t.x; idx=(idx+q)&(S_-1);}
    float w_r=bf2f(wr[q*DF+k]), w_i=bf2f(wi[q*DF+k]); b[q]=make_float2(re*w_r-im*w_i, re*w_i+im*w_r);}
  __syncthreads();
  for(int s=threadIdx.x;s<S_;s+=blockDim.x){ float re=0,im=0; int idx=0; for(int q=0;q<S_;q++){ float2 t=tw[idx]; re+=b[q].x*t.x+b[q].y*t.y; im+=-b[q].x*t.y+b[q].y*t.x; idx=(idx+s)&(S_-1);} X[k*S_+s]=make_float2(re/S_,im/S_);}
}
__global__ void k_irow(const float2* X, float* sout){
  int r=blockIdx.x; __shared__ float2 row[DF]; __shared__ float2 tw[D_];
  for(int i=threadIdx.x;i<D_;i+=blockDim.x){ if(i<DF) row[i]=X[i*S_+r]; float sn,cs; sincospif(2.0f*i/D_,&sn,&cs); tw[i]=make_float2(cs,sn);}
  __syncthreads();
  for(int d=threadIdx.x;d<D_;d+=blockDim.x){ float acc=row[0].x + ((d&1)?-row[320].x:row[320].x); int idx=d; float a2=0; for(int k=1;k<320;k++){ a2+=row[k].x*tw[idx].x-row[k].y*tw[idx].y; idx+=d; if(idx>=D_) idx-=D_; } sout[r*D_+d]=rbf((acc+2*a2)/D_);}
}
__global__ void k_g1(const float* xn, const u16* w1, const u16* b1, float* h){
  int r=blockIdx.x; __shared__ float row[D_];
  for(int i=threadIdx.x;i<D_;i+=blockDim.x) row[i]=xn[r*D_+i];
  __syncthreads();
  for(int f=threadIdx.x;f<F_;f+=blockDim.x){ float a=0; for(int d=0;d<D_;d++) a+=row[d]*bf2f(w1[d*F_+f]); a=rbf(rbf(a)+bf2f(b1[f])); float u=0.7978845608028654f*(a+0.044715f*a*a*a); h[r*F_+f]=rbf(0.5f*a*(1.f+tanhf(u))); }
}
__global__ void k_g2(const float* h, const u16* w2, const u16* b2, const float* s, const u16* x, u16* out){
  int r=blockIdx.x; __shared__ float row[F_];
  for(int i=threadIdx.x;i<F_;i+=blockDim.x) row[i]=h[r*F_+i];
  __syncthreads();
  for(int d=threadIdx.x;d<D_;d+=blockDim.x){ float a=0; for(int f=0;f<F_;f++) a+=row[f]*bf2f(w2[f*D_+d]); a=rbf(rbf(a)+bf2f(b2[d])); float g=rbf(a/(1.f+expf(-a))); out[r*D_+d]=f2bf(bf2f(x[r*D_+d])+rbf(s[r*D_+d]*g)); }
}
extern "C" void kernel_launch(void* const* d_in, const int* in_sizes, int n_in, void* d_out, int out_size, void* d_ws, size_t ws_size, hipStream_t stream){
  const u16* x=(const u16*)d_in[0]; const u16* wr=(const u16*)d_in[1]; const u16* wi=(const u16*)d_in[2];
  const u16* w1=(const u16*)d_in[4]; const u16* b1=(const u16*)d_in[5]; const u16* w2=(const u16*)d_in[6]; const u16* b2=(const u16*)d_in[7];
  char* ws=(char*)d_ws;
  float* xn=(float*)ws; float2* X=(float2*)(ws+(8<<20)); float* s=(float*)(ws+(16<<20)); float* h=(float*)(ws+(24<<20));
  k_ln<<<S_,256,0,stream>>>(x,xn);
  k_rowdft<<<S_,320,0,stream>>>(xn,X);
  k_col<<<DF,1024,0,stream>>>(X,wr,wi);
  k_irow<<<S_,320,0,stream>>>(X,s);
  k_g1<<<S_,256,0,stream>>>(xn,w1,b1,h);
  k_g2<<<S_,320,0,stream>>>(h,w2,b2,s,x,(u16*)d_out);
}
```

```cpp
#define RELEASE
#define W_EARLY
#define USE_PK
#define THROTTLE
#define FPA 0
#define FPB 0
#include <hip/hip_runtime.h>
#include <stdint.h>

typedef unsigned u32; typedef unsigned long long u64; typedef unsigned short u16;
typedef __attribute__((ext_vector_type(2))) float f2;
typedef __attribute__((ext_vector_type(4))) float f4;
typedef __attribute__((ext_vector_type(2))) u32 u2;
typedef __attribute__((ext_vector_type(4))) u32 u4;
typedef __attribute__((ext_vector_type(2))) _Float16 h2;
typedef __attribute__((ext_vector_type(8))) _Float16 h8;
typedef __attribute__((ext_vector_type(2))) __bf16 b2;
typedef __attribute__((ext_vector_type(8))) __bf16 b8;
typedef __attribute__((ext_vector_type(4))) short s4;
typedef __attribute__((ext_vector_type(8))) short s8;
#define LAS __attribute__((address_space(3)))
#define GAS __attribute__((address_space(1)))
#define DEV __device__ __forceinline__

constexpr u32 WS_FW = 0x60000;
constexpr u32 WS_CS = 0x50000;
constexpr u32 WS_FH = 0x68000, WS_FS = 0x70000;
constexpr u32 WS_EPOCH  = 0x8000;
constexpr u32 WS_STATS  = 0x10000;
constexpr u32 WS_T      = 0x100000;
constexpr u32 WS_Y      = 0x500000;
constexpr u32 WS_S      = 0x900000;
constexpr u32 WS_H      = 0x1000000;
constexpr u32 WS_W2T    = 0x1800000;
constexpr u32 WS_END    = 0x1C00000;
constexpr u32 L_TABA = 131072, L_R1 = 139264, L_R2 = 150528, L_TABB = L_R2 + 256, L_STAT = 161792, L_CS = 162816, L_B1 = 163328, L_CNT = 163584;
constexpr u32 C_FULL = L_CNT  , C_FREE = L_CNT + 16  , C_TAG = L_CNT + 32  , C_F = L_CNT + 40, C_G = L_CNT + 44, C_TAGV = L_CNT + 48,
              C_STAT = L_CNT + 52, C_P1 = L_CNT + 56, C_HOK = L_CNT + 60, C_SOK = L_CNT + 64, C_P0 = L_CNT + 68, C_WOK = L_CNT + 72, C_P2 = L_CNT + 76, C_LD = L_CNT + 80;
constexpr int SPIN_MAX = 1 << 17;

__device__ const u32 __attribute__((aligned(1024))) g_tab[5120] = {
0x3400,0x3400,0x3400,0x3400,0x3400,0x2e1f3364,0x31a831a8,0x33642e1f,0x3400,0x31a831a8,0x34000000,0x31a8b1a8,
0x3400,0x33642e1f,0x31a8b1a8,0xae1fb364,0x3400,0x34000000,0xb400,0xb4008000,0x3400,0x3364ae1f,0xb1a8b1a8,0xae1f3364,
0x3400,0x31a8b1a8,0xb4008000,0x31a831a8,0x3400,0x2e1fb364,0xb1a831a8,0x3364ae1f,0x3400,0xb400,0x3400,0xb400,
0x3400,0xae1fb364,0x31a831a8,0xb364ae1f,0x3400,0xb1a8b1a8,0x34000000,0xb1a831a8,0x3400,0xb364ae1f,0x31a8b1a8,0x2e1f3364,
0x3400,0xb4008000,0xb400,0x34000000,0x3400,0xb3642e1f,0xb1a8b1a8,0x2e1fb364,0x3400,0xb1a831a8,0xb4008000,0xb1a8b1a8,
0x3400,0xae1f3364,0xb1a831a8,0xb3642e1f,0x3400,0x3400,0x3400,0x3400,0x34000000,0x3364ae1f,0x31a8b1a8,0x2e1fb364,
0xb400,0xb1a8b1a8,0xb4008000,0xb1a831a8,0xb4008000,0xae1f3364,0x31a831a8,0x3364ae1f,0x3400,0x34000000,0xb400,0xb4008000,
0x34000000,0xae1fb364,0xb1a831a8,0x33642e1f,0xb400,0xb1a831a8,0x34000000,0xb1a8b1a8,0xb4008000,0x33642e1f,0xb1a8b1a8,0x2e1f3364,
0x3400,0xb400,0x3400,0xb400,0x34000000,0xb3642e1f,0x31a8b1a8,0xae1f3364,0xb400,0x31a831a8,0xb4008000,0x31a8b1a8,
0xb4008000,0x2e1fb364,0x31a831a8,0xb3642e1f,0x3400,0xb4008000,0xb400,0x34000000,0x34000000,0x2e1f3364,0xb1a831a8,0xb364ae1f,
0xb400,0x31a8b1a8,0x34000000,0x31a831a8,0xb4008000,0xb364ae1f,0xb1a8b1a8,0xae1fb364,0x3400,0x3400,0x3400,0x3400,
0xb400,0xae1fb364,0xb1a8b1a8,0xb364ae1f,0x3400,0x31a831a8,0x34000000,0x31a8b1a8,0xb400,0xb364ae1f,0xb1a831a8,0x2e1f3364,
0x3400,0x34000000,0xb400,0xb4008000,0xb400,0xb3642e1f,0x31a831a8,0x2e1fb364,0x3400,0x31a8b1a8,0xb4008000,0x31a831a8,
0xb400,0xae1f3364,0x31a8b1a8,0xb3642e1f,0x3400,0xb400,0x3400,0xb400,0xb400,0x2e1f3364,0xb1a8b1a8,0x33642e1f,
0x3400,0xb1a8b1a8,0x34000000,0xb1a831a8,0xb400,0x33642e1f,0xb1a831a8,0xae1fb364,0x3400,0xb4008000,0xb400,0x34000000,
0xb400,0x3364ae1f,0x31a831a8,0xae1f3364,0x3400,0xb1a831a8,0xb4008000,0xb1a8b1a8,0xb400,0x2e1fb364,0x31a8b1a8,0x3364ae1f,
0x3400,0x3400,0x3400,0x3400,0xb4008000,0xb3642e1f,0xb1a831a8,0xae1f3364,0xb400,0xb1a8b1a8,0xb4008000,0xb1a831a8,
0x34000000,0x2e1fb364,0xb1a8b1a8,0xb3642e1f,0x3400,0x34000000,0xb400,0xb4008000,0xb4008000,0x2e1f3364,0x31a8b1a8,0xb364ae1f,
0xb400,0xb1a831a8,0x34000000,0xb1a8b1a8,0x34000000,0xb364ae1f,0x31a831a8,0xae1fb364,0x3400,0xb400,0x3400,0xb400,
0xb4008000,0x3364ae1f,0xb1a831a8,0x2e1fb364,0xb400,0x31a831a8,0xb4008000,0x31a8b1a8,0x34000000,0xae1f3364,0xb1a8b1a8,0x3364ae1f,
0x3400,0xb4008000,0xb400,0x34000000,0xb4008000,0xae1fb364,0x31a8b1a8,0x33642e1f,0xb400,0x31a8b1a8,0x34000000,0x31a831a8,
0x34000000,0x33642e1f,0x31a831a8,0x2e1f3364,0x34008000,0x34008000,0x34008000,0x34008000,0x34008000,0x3364ae1f,0x31a8b1a8,0x2e1fb364,
0x34008000,0x31a8b1a8,0xb400,0xb1a8b1a8,0x34008000,0x2e1fb364,0xb1a8b1a8,0xb3642e1f,0x34008000,0xb400,0xb4008000,0x80003400,
0x34008000,0xae1fb364,0xb1a831a8,0x33642e1f,0x34008000,0xb1a8b1a8,0x80003400,0x31a8b1a8,0x34008000,0xb364ae1f,0x31a831a8,0xae1fb364,
0x34008000,0xb4008000,0x34008000,0xb4008000,0x34008000,0xb3642e1f,0x31a8b1a8,0xae1f3364,0x34008000,0xb1a831a8,0xb400,0x31a831a8,
0x34008000,0xae1f3364,0xb1a8b1a8,0x3364ae1f,0x34008000,0x80003400,0xb4008000,0xb400,0x34008000,0x2e1f3364,0xb1a831a8,0xb364ae1f,
0x34008000,0x31a831a8,0x80003400,0xb1a831a8,0x34008000,0x33642e1f,0x31a831a8,0x2e1f3364,0x34008000,0x34008000,0x34008000,0x34008000,
0xb400,0xae1fb364,0xb1a8b1a8,0xb364ae1f,0xb4008000,0xb1a831a8,0x80003400,0x31a831a8,0x80003400,0x33642e1f,0x31a8b1a8,0xae1fb364,
0x34008000,0xb400,0xb4008000,0x80003400,0xb400,0xb3642e1f,0x31a831a8,0x2e1fb364,0xb4008000,0x31a831a8,0xb400,0xb1a831a8,
0x80003400,0x2e1fb364,0xb1a831a8,0x3364ae1f,0x34008000,0xb4008000,0x34008000,0xb4008000,0xb400,0x2e1f3364,0xb1a8b1a8,0x33642e1f,
0xb4008000,0x31a8b1a8,0x80003400,0xb1a8b1a8,0x80003400,0xb364ae1f,0x31a8b1a8,0x2e1f3364,0x34008000,0x80003400,0xb4008000,0xb400,
0xb400,0x3364ae1f,0x31a831a8,0xae1f3364,0xb4008000,0xb1a8b1a8,0xb400,0x31a8b1a8,0x80003400,0xae1f3364,0xb1a831a8,0xb3642e1f,
0x34008000,0x34008000,0x34008000,0x34008000,0xb4008000,0xb3642e1f,0xb1a831a8,0xae1f3364,0x34008000,0x31a8b1a8,0xb400,0xb1a8b1a8,
0xb4008000,0xae1f3364,0x31a831a8,0x3364ae1f,0x34008000,0xb400,0xb4008000,0x80003400,0xb4008000,0x2e1f3364,0x31a8b1a8,0xb364ae1f,
0x34008000,0xb1a8b1a8,0x80003400,0x31a8b1a8,0xb4008000,0x33642e1f,0xb1a8b1a8,0x2e1f3364,0x34008000,0xb4008000,0x34008000,0xb4008000,
0xb4008000,0x3364ae1f,0xb1a831a8,0x2e1fb364,0x34008000,0xb1a831a8,0xb400,0x31a831a8,0xb4008000,0x2e1fb364,0x31a831a8,0xb3642e1f,
0x34008000,0x80003400,0xb4008000,0xb400,0xb4008000,0xae1fb364,0x31a8b1a8,0x33642e1f,0x34008000,0x31a831a8,0x80003400,0xb1a831a8,
0xb4008000,0xb364ae1f,0xb1a8b1a8,0xae1fb364,0x34008000,0x34008000,0x34008000,0x34008000,0x80003400,0x2e1f3364,0x31a831a8,0x33642e1f,
0xb4008000,0xb1a831a8,0x80003400,0x31a831a8,0xb400,0xb364ae1f,0xb1a831a8,0x2e1f3364,0x34008000,0xb400,0xb4008000,0x80003400,
0x80003400,0x3364ae1f,0xb1a8b1a8,0xae1f3364,0xb4008000,0x31a831a8,0xb400,0xb1a831a8,0xb400,0xae1f3364,0x31a8b1a8,0xb3642e1f,
0x34008000,0xb4008000,0x34008000,0xb4008000,0x80003400,0xae1fb364,0x31a831a8,0xb364ae1f,0xb4008000,0x31a8b1a8,0x80003400,0xb1a8b1a8,
0xb400,0x33642e1f,0xb1a831a8,0xae1fb364,0x34008000,0x80003400,0xb4008000,0xb400,0x80003400,0xb3642e1f,0xb1a8b1a8,0x2e1fb364,
0xb4008000,0xb1a8b1a8,0xb400,0x31a8b1a8,0xb400,0x2e1fb364,0x31a8b1a8,0x3364ae1f,0x310f,0x310f,0x310f,0x310f,
0x310f,0x265530ff,0x2a4130d0,0x2c983082,0x310f,0x2a4130d0,0x2df33018,0x30182df3,0x310f,0x2c983082,0x30182df3,0x30ff2655,
0x310f,0x2df33018,0x30d02a41,0x30d0aa41,0x310f,0x2f282f28,0x310f0000,0x2f28af28,0x310f,0x30182df3,0x30d0aa41,0x2a41b0d0,
0x310f,0x30822c98,0x3018adf3,0xa655b0ff,0x310f,0x30d02a41,0x2df3b018,0xadf3b018,0x310f,0x30ff2655,0x2a41b0d0,0xb082ac98,
0x310f,0x310f0000,0xb10f,0xb10f8000,0x310f,0x30ffa655,0xaa41b0d0,0xb0822c98,0x310f,0x30d0aa41,0xadf3b018,0xadf33018,
0x310f,0x3082ac98,0xb018adf3,0xa65530ff,0x310f,0x3018adf3,0xb0d0aa41,0x2a4130d0,0x310f,0x2f28af28,0xb10f8000,0x2f282f28,
0x310f,0x310f,0x310f,0x310f,0x2df33018,0x2f282f28,0x30182df3,0x30822c98,0x30d02a41,0x310f0000,0x30d0aa41,0x3018adf3,
0x30d0aa41,0x2f28af28,0x2a41b0d0,0xa655b0ff,0x2df3b018,0xb10f,0xadf3b018,0xb0d0aa41,0xb10f,0xaf28af28,0xb10f8000,0xaf282f28,
0xadf3b018,0xb10f8000,0xadf33018,0x2a4130d0,0xb0d0aa41,0xaf282f28,0x2a4130d0,0x30ff2655,0xb0d02a41,0x310f,0x30d02a41,0x2df3b018,
0xadf33018,0x2f282f28,0x3018adf3,0xac98b082,0x310f,0x310f0000,0xb10f,0xb10f8000,0x2df33018,0x2f28af28,0xb018adf3,0xac983082,
0x30d02a41,0xb10f,0xb0d02a41,0x2df33018,0x30d0aa41,0xaf28af28,0xaa4130d0,0x30ffa655,0x2df3b018,0xb10f8000,0x2df33018,0x2a41b0d0,
0xb10f,0xaf282f28,0x310f0000,0xaf28af28,0x310f,0x310f,0x310f,0x310f,0x30d02a41,0x30ff2655,0x310f0000,0x30ffa655,
0x2df3b018,0x2a41b0d0,0xb10f,0xaa41b0d0,0xadf3b018,0xb082ac98,0xb10f8000,0xb0822c98,0xb0d02a41,0xadf33018,0x310f,0x2df33018,
0x310f,0x2f282f28,0x310f0000,0x2f28af28,0x30d02a41,0x3018adf3,0xb10f,0xb018adf3,0x2df3b018,0xac98b082,0xb10f8000,0xac983082,
0xadf3b018,0xb0d02a41,0x310f,0x30d02a41,0xb0d02a41,0x265530ff,0x310f0000,0x2655b0ff,0x310f,0x310f0000,0xb10f,0xb10f8000,
0x30d02a41,0x2655b0ff,0xb10f8000,0x265530ff,0x2df3b018,0xb0d0aa41,0x310f,0x30d0aa41,0xadf3b018,0xac983082,0x310f0000,0xac98b082,
0xb0d02a41,0x30182df3,0xb10f,0xb0182df3,0x310f,0x2f28af28,0xb10f8000,0x2f282f28,0x310f,0x310f,0x310f,0x310f,
0x30d0aa41,0x3082ac98,0x3018adf3,0x2f28af28,0xadf3b018,0xb018adf3,0xb0d0aa41,0xb10f8000,0xadf33018,0xa65530ff,0x2a4130d0,0x2f282f28,
0x30d02a41,0x30d0aa41,0x2df3b018,0xb10f,0xb10f,0xaf28af28,0xb10f8000,0xaf282f28,0xb0d02a41,0xaa4130d0,0x2df33018,0x310f0000,
0x2df33018,0x30ffa655,0x2a41b0d0,0xaf28af28,0x2df3b018,0xadf3b018,0xb0d02a41,0x310f,0xb0d0aa41,0xac983082,0x30182df3,0x2f28af28,
0x310f,0x310f0000,0xb10f,0xb10f8000,0x30d0aa41,0xac98b082,0xb0182df3,0x2f282f28,0xadf3b018,0xadf33018,0x30d02a41,0xb10f,
0xadf33018,0x30ff2655,0xaa41b0d0,0xaf282f28,0x30d02a41,0xaa41b0d0,0xadf33018,0x310f0000,0xb10f,0xaf282f28,0x310f0000,0xaf28af28,
0x310f8000,0x310f8000,0x310f8000,0x310f8000,0x310f8000,0x30ffa655,0x30d0aa41,0x3082ac98,0x310f8000,0x30d0aa41,0x3018adf3,0x2df3b018,
0x310f8000,0x3082ac98,0x2df3b018,0x2655b0ff,0x310f8000,0x3018adf3,0x2a41b0d0,0xaa41b0d0,0x310f8000,0x2f28af28,0xb10f,0xaf28af28,
0x310f8000,0x2df3b018,0xaa41b0d0,0xb0d0aa41,0x310f8000,0x2c98b082,0xadf3b018,0xb0ff2655,0x310f8000,0x2a41b0d0,0xb018adf3,0xb0182df3,
0x310f8000,0x2655b0ff,0xb0d0aa41,0xac983082,0x310f8000,0xb10f,0xb10f8000,0x8000310f,0x310f8000,0xa655b0ff,0xb0d02a41,0x2c983082,
0x310f8000,0xaa41b0d0,0xb0182df3,0x30182df3,0x310f8000,0xac98b082,0xadf33018,0x30ff2655,0x310f8000,0xadf3b018,0xaa4130d0,0x30d0aa41,
0x310f8000,0xaf28af28,0x8000310f,0x2f28af28,0x310f8000,0x310f8000,0x310f8000,0x310f8000,0x3018adf3,0x2f28af28,0x2df3b018,0x2c98b082,
0x2a41b0d0,0xb10f,0xaa41b0d0,0xadf3b018,0xaa41b0d0,0xaf28af28,0xb0d0aa41,0xb0ff2655,0xb018adf3,0xb10f8000,0xb0182df3,0xaa4130d0,
0xb10f8000,0xaf282f28,0x8000310f,0x2f282f28,0xb0182df3,0x8000310f,0x30182df3,0x30d0aa41,0xaa4130d0,0x2f282f28,0x30d0aa41,0x2655b0ff,
0x2a4130d0,0x310f8000,0x2a41b0d0,0xb018adf3,0x30182df3,0x2f28af28,0xadf3b018,0xb0822c98,0x310f8000,0xb10f,0xb10f8000,0x8000310f,
0x3018adf3,0xaf28af28,0xadf33018,0x30822c98,0x2a41b0d0,0xb10f8000,0x2a4130d0,0x3018adf3,0xaa41b0d0,0xaf282f28,0x30d02a41,0xa655b0ff,
0xb018adf3,0x8000310f,0x3018adf3,0xb0d0aa41,0xb10f8000,0x2f282f28,0xb10f,0xaf282f28,0x310f8000,0x310f8000,0x310f8000,0x310f8000,
0x2a41b0d0,0x2655b0ff,0xb10f,0xa655b0ff,0xb018adf3,0xb0d0aa41,0xb10f8000,0xb0d02a41,0xb0182df3,0xac983082,0x8000310f,0x2c983082,
0x2a4130d0,0x30182df3,0x310f8000,0x3018adf3,0x310f8000,0x2f28af28,0xb10f,0xaf28af28,0x2a41b0d0,0xadf3b018,0xb10f8000,0xadf33018,
0xb018adf3,0xb0822c98,0x8000310f,0x30822c98,0xb0182df3,0x2a4130d0,0x310f8000,0x2a41b0d0,0x2a4130d0,0x30ffa655,0xb10f,0xb0ffa655,
0x310f8000,0xb10f,0xb10f8000,0x8000310f,0x2a41b0d0,0xb0ffa655,0x8000310f,0x30ffa655,0xb018adf3,0xaa4130d0,0x310f8000,0xaa41b0d0,
0xb0182df3,0x30822c98,0xb10f,0xb0822c98,0x2a4130d0,0x2df3b018,0xb10f8000,0x2df33018,0x310f8000,0xaf28af28,0x8000310f,0x2f28af28,
0x310f8000,0x310f8000,0x310f8000,0x310f8000,0xaa41b0d0,0xac98b082,0xadf3b018,0xaf28af28,0xb0182df3,0xadf33018,0xaa4130d0,0x8000310f,
0x30182df3,0x30ff2655,0x30d0aa41,0x2f28af28,0x2a41b0d0,0xaa41b0d0,0xb018adf3,0xb10f8000,0xb10f8000,0xaf282f28,0x8000310f,0x2f282f28,
0x2a4130d0,0x30d02a41,0x3018adf3,0xb10f,0x3018adf3,0xa655b0ff,0xb0d0aa41,0xaf282f28,0xb018adf3,0xb0182df3,0x2a4130d0,0x310f8000,
0xaa4130d0,0x30822c98,0x2df3b018,0xaf28af28,0x310f8000,0xb10f,0xb10f8000,0x8000310f,0xaa41b0d0,0xb0822c98,0x2df33018,0x2f28af28,
0xb0182df3,0x30182df3,0x2a41b0d0,0xb10f8000,0x30182df3,0x2655b0ff,0xb0d02a41,0x2f282f28,0x2a41b0d0,0xb0d02a41,0x30182df3,0xb10f,
0xb10f8000,0x2f282f28,0xb10f,0xaf282f28,0x310f,0x310f,0x310f,0x310f,0x2df3b018,0x2c98b082,0x2a41b0d0,0x2655b0ff,
0xb0d02a41,0xb0182df3,0xadf33018,0xaa4130d0,0x30d02a41,0x30ffa655,0x3018adf3,0x2c98b082,0xadf3b018,0xb0d0aa41,0xb0d02a41,0xadf33018,
0x310f,0x2f282f28,0x310f0000,0x2f28af28,0x2df3b018,0xaa41b0d0,0xb0d0aa41,0xb0182df3,0xb0d02a41,0xa65530ff,0x30182df3,0x3082ac98,
0x30d02a41,0x2df3b018,0xadf3b018,0xb0d02a41,0xadf3b018,0xb0822c98,0x2a4130d0,0x30ffa655,0x310f,0x310f0000,0xb10f,0xb10f8000,
0x2df3b018,0xb082ac98,0xaa4130d0,0x30ff2655,0xb0d02a41,0x2df33018,0x2df3b018,0xb0d0aa41,0x30d02a41,0xa655b0ff,0xb0182df3,0x30822c98,
0xadf3b018,0xaa4130d0,0x30d0aa41,0xb018adf3,0x310f,0x2f28af28,0xb10f8000,0x2f282f28,0x310f,0x310f,0x310f,0x310f,
0xb10f,0xa655b0ff,0xaa41b0d0,0xac98b082,0x310f,0x2a4130d0,0x2df33018,0x30182df3,0xb10f,0xac98b082,0xb018adf3,0xb0ffa655,
0x310f,0x2df33018,0x30d02a41,0x30d0aa41,0xb10f,0xaf28af28,0xb10f8000,0xaf282f28,0x310f,0x30182df3,0x30d0aa41,0x2a41b0d0,
0xb10f,0xb082ac98,0xb0182df3,0x265530ff,0x310f,0x30d02a41,0x2df3b018,0xadf3b018,0xb10f,0xb0ffa655,0xaa4130d0,0x30822c98,
0x310f,0x310f0000,0xb10f,0xb10f8000,0xb10f,0xb0ff2655,0x2a4130d0,0x3082ac98,0x310f,0x30d0aa41,0xadf3b018,0xadf33018,
0xb10f,0xb0822c98,0x30182df3,0x2655b0ff,0x310f,0x3018adf3,0xb0d0aa41,0x2a4130d0,0xb10f,0xaf282f28,0x310f0000,0xaf28af28,
0x310f,0x310f,0x310f,0x310f,0xadf3b018,0xaf28af28,0xb018adf3,0xb082ac98,0x30d02a41,0x310f0000,0x30d0aa41,0x3018adf3,
0xb0d02a41,0xaf282f28,0xaa4130d0,0x265530ff,0x2df3b018,0xb10f,0xadf3b018,0xb0d0aa41,0x310f,0x2f282f28,0x310f0000,0x2f28af28,
0xadf3b018,0xb10f8000,0xadf33018,0x2a4130d0,0x30d02a41,0x2f28af28,0xaa41b0d0,0xb0ffa655,0xb0d02a41,0x310f,0x30d02a41,0x2df3b018,
0x2df3b018,0xaf28af28,0xb0182df3,0x2c983082,0x310f,0x310f0000,0xb10f,0xb10f8000,0xadf3b018,0xaf282f28,0x30182df3,0x2c98b082,
0x30d02a41,0xb10f,0xb0d02a41,0x2df33018,0xb0d02a41,0x2f282f28,0x2a41b0d0,0xb0ff2655,0x2df3b018,0xb10f8000,0x2df33018,0x2a41b0d0,
0x310f,0x2f28af28,0xb10f8000,0x2f282f28,0x310f,0x310f,0x310f,0x310f,0xb0d0aa41,0xb0ffa655,0xb10f8000,0xb0ff2655,
0x2df3b018,0x2a41b0d0,0xb10f,0xaa41b0d0,0x2df33018,0x30822c98,0x310f0000,0x3082ac98,0xb0d02a41,0xadf33018,0x310f,0x2df33018,
0xb10f,0xaf28af28,0xb10f8000,0xaf282f28,0x30d02a41,0x3018adf3,0xb10f,0xb018adf3,0xadf33018,0x2c983082,0x310f0000,0x2c98b082,
0xadf3b018,0xb0d02a41,0x310f,0x30d02a41,0x30d0aa41,0xa655b0ff,0xb10f8000,0xa65530ff,0x310f,0x310f0000,0xb10f,0xb10f8000,
0xb0d0aa41,0xa65530ff,0x310f0000,0xa655b0ff,0x2df3b018,0xb0d0aa41,0x310f,0x30d0aa41,0x2df33018,0x2c98b082,0xb10f8000,0x2c983082,
0xb0d02a41,0x30182df3,0xb10f,0xb0182df3,0xb10f,0xaf282f28,0x310f0000,0xaf28af28,0x310f8000,0x310f8000,0x310f8000,0x310f8000,
0xb018adf3,0xb082ac98,0xb0d0aa41,0xb0ffa655,0x2a4130d0,0x2df33018,0x30182df3,0x30d02a41,0x2a41b0d0,0xa655b0ff,0xadf3b018,0xb082ac98,
0xb0182df3,0xaa4130d0,0x2a4130d0,0x30182df3,0x310f8000,0x2f28af28,0xb10f,0xaf28af28,0xb018adf3,0xb0d02a41,0xaa4130d0,0x2df33018,
0x2a4130d0,0x30ff2655,0x2df3b018,0xac98b082,0x2a41b0d0,0xb018adf3,0xb0182df3,0x2a4130d0,0xb0182df3,0x2c983082,0x30d0aa41,0xa655b0ff,
0x310f8000,0xb10f,0xb10f8000,0x8000310f,0xb018adf3,0xac983082,0x30d02a41,0x2655b0ff,0x2a4130d0,0x3018adf3,0xb018adf3,0xaa4130d0,
0x2a41b0d0,0xb0ff2655,0x2df33018,0x2c98b082,0xb0182df3,0x30d02a41,0xaa41b0d0,0xadf33018,0x310f8000,0xaf28af28,0x8000310f,0x2f28af28,
0x310f8000,0x310f8000,0x310f8000,0x310f8000,0xb10f8000,0xb0ff2655,0xb0d02a41,0xb0822c98,0x310f8000,0x30d0aa41,0x3018adf3,0x2df3b018,
0xb10f8000,0xb0822c98,0xadf33018,0xa65530ff,0x310f8000,0x3018adf3,0x2a41b0d0,0xaa41b0d0,0xb10f8000,0xaf282f28,0x8000310f,0x2f282f28,
0x310f8000,0x2df3b018,0xaa41b0d0,0xb0d0aa41,0xb10f8000,0xac983082,0x2df33018,0x30ffa655,0x310f8000,0x2a41b0d0,0xb018adf3,0xb0182df3,
0xb10f8000,0xa65530ff,0x30d02a41,0x2c98b082,0x310f8000,0xb10f,0xb10f8000,0x8000310f,0xb10f8000,0x265530ff,0x30d0aa41,0xac98b082,
0x310f8000,0xaa41b0d0,0xb0182df3,0x30182df3,0xb10f8000,0x2c983082,0x2df3b018,0xb0ffa655,0x310f8000,0xadf3b018,0xaa4130d0,0x30d0aa41,
0xb10f8000,0x2f282f28,0xb10f,0xaf282f28,0x310f8000,0x310f8000,0x310f8000,0x310f8000,0xb0182df3,0xaf282f28,0xadf33018,0xac983082,
0x2a41b0d0,0xb10f,0xaa41b0d0,0xadf3b018,0x2a4130d0,0x2f282f28,0x30d02a41,0x30ffa655,0xb018adf3,0xb10f8000,0xb0182df3,0xaa4130d0,
0x310f8000,0x2f28af28,0xb10f,0xaf28af28,0xb0182df3,0x8000310f,0x30182df3,0x30d0aa41,0x2a41b0d0,0xaf28af28,0xb0d02a41,0xa65530ff,
0x2a4130d0,0x310f8000,0x2a41b0d0,0xb018adf3,0xb018adf3,0xaf282f28,0x2df33018,0x3082ac98,0x310f8000,0xb10f,0xb10f8000,0x8000310f,
0xb0182df3,0x2f282f28,0x2df3b018,0xb082ac98,0x2a41b0d0,0xb10f8000,0x2a4130d0,0x3018adf3,0x2a4130d0,0x2f28af28,0xb0d0aa41,0x265530ff,
0xb018adf3,0x8000310f,0x3018adf3,0xb0d0aa41,0x310f8000,0xaf28af28,0x8000310f,0x2f28af28,0x310f8000,0x310f8000,0x310f8000,0x310f8000,
0xaa4130d0,0xa65530ff,0x8000310f,0x265530ff,0xb018adf3,0xb0d0aa41,0xb10f8000,0xb0d02a41,0x3018adf3,0x2c98b082,0xb10f,0xac98b082,
0x2a4130d0,0x30182df3,0x310f8000,0x3018adf3,0xb10f8000,0xaf282f28,0x8000310f,0x2f282f28,0x2a41b0d0,0xadf3b018,0xb10f8000,0xadf33018,
0x30182df3,0x3082ac98,0xb10f,0xb082ac98,0xb0182df3,0x2a4130d0,0x310f8000,0x2a41b0d0,0xaa41b0d0,0xb0ff2655,0x8000310f,0x30ff2655,
0x310f8000,0xb10f,0xb10f8000,0x8000310f,0xaa4130d0,0x30ff2655,0xb10f,0xb0ff2655,0xb018adf3,0xaa4130d0,0x310f8000,0xaa41b0d0,
0x3018adf3,0xb082ac98,0x8000310f,0x3082ac98,0x2a4130d0,0x2df3b018,0xb10f8000,0x2df33018,0xb10f8000,0x2f282f28,0xb10f,0xaf282f28,
0x310f,0x310f,0x310f,0x310f,0xb0d02a41,0xb0822c98,0xb0182df3,0xaf282f28,0xadf3b018,0xb018adf3,0xb0d0aa41,0xb10f8000,
0x2df3b018,0x2655b0ff,0xaa41b0d0,0xaf28af28,0x30d02a41,0x30d0aa41,0x2df3b018,0xb10f,0x310f,0x2f282f28,0x310f0000,0x2f28af28,
0xb0d02a41,0xaa4130d0,0x2df33018,0x310f0000,0xadf3b018,0xb0ff2655,0xaa4130d0,0x2f282f28,0x2df3b018,0xadf3b018,0xb0d02a41,0x310f,
0x30d02a41,0x2c98b082,0xb018adf3,0xaf282f28,0x310f,0x310f0000,0xb10f,0xb10f8000,0xb0d02a41,0x2c983082,0x3018adf3,0xaf28af28,
0xadf3b018,0xadf33018,0x30d02a41,0xb10f,0x2df3b018,0xb0ffa655,0x2a4130d0,0x2f28af28,0x30d02a41,0xaa41b0d0,0xadf33018,0x310f0000,
0x310f,0x2f28af28,0xb10f8000,0x2f282f28,0x310f,0x310f,0x310f,0x310f,0xadf33018,0xac983082,0xaa4130d0,0xa65530ff,
0xb0d02a41,0xb0182df3,0xadf33018,0xaa4130d0,0xb0d0aa41,0xb0ff2655,0xb0182df3,0xac983082,0xadf3b018,0xb0d0aa41,0xb0d02a41,0xadf33018,
0xb10f,0xaf28af28,0xb10f8000,0xaf282f28,0x2df3b018,0xaa41b0d0,0xb0d0aa41,0xb0182df3,0x30d0aa41,0x2655b0ff,0xb018adf3,0xb0822c98,
0x30d02a41,0x2df3b018,0xadf3b018,0xb0d02a41,0x2df33018,0x3082ac98,0xaa41b0d0,0xb0ff2655,0x310f,0x310f0000,0xb10f,0xb10f8000,
0xadf33018,0x30822c98,0x2a41b0d0,0xb0ffa655,0xb0d02a41,0x2df33018,0x2df3b018,0xb0d0aa41,0xb0d0aa41,0x265530ff,0x3018adf3,0xb082ac98,
0xadf3b018,0xaa4130d0,0x30d0aa41,0xb018adf3,0xb10f,0xaf282f28,0x310f0000,0xaf28af28,0,0,0,0,
0,0,0,0,0,0,0,0,0,0,0,0,
0,0,0,0,0,0,0,0,0,0,0,0,
0,0,0,0,0,0,0,0,0,0,0,0,
0,0,0,0,0,0,0,0,0,0,0,0,
0,0,0,0,0,0,0,0,0,0,0,0,
0,0,0,0,0,0,0,0,0,0,0,0,
0,0,0,0,0,0,0,0,0,0,0,0,
0,0,0,0,0,0,0,0,0,0,0,0,
0,0,0,0,0,0,0,0,0,0,0,0,
0,0,0,0,0,0,0,0,0,0,0,0,
0,0,0,0,0x310f8000,0x310f8000,0x310f8000,0x310f8000,0x2a4130d0,0x2c983082,0x2df33018,0x2f282f28,
0xb0182df3,0xadf33018,0xaa4130d0,0x8000310f,0xb018adf3,0xb0ffa655,0xb0d02a41,0xaf282f28,0x2a41b0d0,0xaa41b0d0,0xb018adf3,0xb10f8000,
0x310f8000,0x2f28af28,0xb10f,0xaf28af28,0x2a4130d0,0x30d02a41,0x3018adf3,0xb10f,0xb0182df3,0x265530ff,0x30d02a41,0x2f28af28,
0xb018adf3,0xb0182df3,0x2a4130d0,0x310f8000,0x2a41b0d0,0xb082ac98,0xadf33018,0x2f282f28,0x310f8000,0xb10f,0xb10f8000,0x8000310f,
0x2a4130d0,0x3082ac98,0xadf3b018,0xaf282f28,0xb0182df3,0x30182df3,0x2a41b0d0,0xb10f8000,0xb018adf3,0xa65530ff,0x30d0aa41,0xaf28af28,
0x2a41b0d0,0xb0d02a41,0x30182df3,0xb10f,0x310f8000,0xaf28af28,0x8000310f,0x2f28af28,0x310f8000,0x310f8000,0x310f8000,0x310f8000,
0x30182df3,0x30822c98,0x30d02a41,0x30ff2655,0x2a4130d0,0x2df33018,0x30182df3,0x30d02a41,0xaa4130d0,0x265530ff,0x2df33018,0x30822c98,
0xb0182df3,0xaa4130d0,0x2a4130d0,0x30182df3,0xb10f8000,0xaf282f28,0x8000310f,0x2f282f28,0xb018adf3,0xb0d02a41,0xaa4130d0,0x2df33018,
0xaa41b0d0,0xb0ffa655,0xadf33018,0x2c983082,0x2a41b0d0,0xb018adf3,0xb0182df3,0x2a4130d0,0x3018adf3,0xac98b082,0xb0d02a41,0x265530ff,
0x310f8000,0xb10f,0xb10f8000,0x8000310f,0x30182df3,0x2c98b082,0xb0d0aa41,0xa65530ff,0x2a4130d0,0x3018adf3,0xb018adf3,0xaa4130d0,
0xaa4130d0,0x30ffa655,0xadf3b018,0xac983082,0xb0182df3,0x30d02a41,0xaa41b0d0,0xadf33018,0xb10f8000,0x2f282f28,0xb10f,0xaf282f28,
0,0,0,0,0,0,0,0,0,0,0,0,
0,0,0,0,0,0,0,0,0,0,0,0,
0,0,0,0,0,0,0,0,0,0,0,0,
0,0,0,0,0,0,0,0,0,0,0,0,
0,0,0,0,0,0,0,0,0,0,0,0,
0,0,0,0,0,0,0,0,0,0,0,0,
0,0,0,0,0,0,0,0,0,0,0,0,
0,0,0,0,0,0,0,0,0,0,0,0,
0,0,0,0,0,0,0,0,0,0,0,0,
0,0,0,0,0,0,0,0,0,0,0,0,
0,0,0,0,0,0,0,0,0x310f,0x2df3b018,0xb0d02a41,0x30d02a41,
0x310f,0x2c98b082,0xb0182df3,0x30ffa655,0x310f,0x2a41b0d0,0xadf33018,0x3018adf3,0x310f,0x2655b0ff,0xaa4130d0,0x2c98b082,
0x310f,0xb10f,0x310f,0xb10f,0x310f,0xa655b0ff,0x2a4130d0,0xac98b082,0x310f,0xaa41b0d0,0x2df33018,0xb018adf3,
0x310f,0xac98b082,0x30182df3,0xb0ffa655,0x310f,0xadf3b018,0x30d02a41,0xb0d02a41,0x310f,0xaf28af28,0x310f0000,0xaf282f28,
0x310f,0xb018adf3,0x30d0aa41,0xaa4130d0,0x310f,0xb082ac98,0x3018adf3,0x265530ff,0x310f,0xb0d0aa41,0x2df3b018,0x2df33018,
0x310f,0xb0ffa655,0x2a41b0d0,0x30822c98,0x310f,0xb10f8000,0xb10f,0x310f0000,0x310f,0xb0ff2655,0xaa41b0d0,0x3082ac98,
0xadf3b018,0x310f,0x2df3b018,0xb0d02a41,0xb0d0aa41,0x2f282f28,0xaa41b0d0,0xa65530ff,0xb0d02a41,0x310f0000,0xb0d0aa41,0x30182df3,
0xadf33018,0x2f28af28,0xb0182df3,0x3082ac98,0x310f,0xb10f,0x310f,0xb10f,0x2df33018,0xaf28af28,0x30182df3,0xb082ac98,
0x30d02a41,0xb10f8000,0x30d0aa41,0xb0182df3,0x30d0aa41,0xaf282f28,0x2a41b0d0,0x265530ff,0x2df3b018,0x310f,0xadf3b018,0x30d02a41,
0xb10f,0x2f282f28,0xb10f8000,0x2f28af28,0xadf3b018,0x310f0000,0xadf33018,0xaa41b0d0,0xb0d0aa41,0x2f28af28,0x2a4130d0,0xb0ffa655,
0xb0d02a41,0xb10f,0x30d02a41,0xadf33018,0xadf33018,0xaf28af28,0x3018adf3,0x2c983082,0x310f,0xb10f8000,0xb10f,0x310f0000,
0x2df33018,0xaf282f28,0xb018adf3,0x2c98b082,0x30d02a41,0xadf3b018,0x310f,0x2df3b018,0x2df3b018,0xb0822c98,0x310f0000,0xb082ac98,
0xadf3b018,0x2a4130d0,0xb10f,0xaa4130d0,0xb0d02a41,0x30ffa655,0xb10f8000,0x30ff2655,0x310f,0xb10f,0x310f,0xb10f,
0x30d02a41,0xb0ffa655,0x310f0000,0xb0ff2655,0x2df3b018,0xaa4130d0,0xb10f,0x2a4130d0,0xadf3b018,0x30822c98,0xb10f8000,0x3082ac98,
0xb0d02a41,0x2df3b018,0x310f,0xadf3b018,0x310f,0xaf28af28,0x310f0000,0xaf282f28,0x30d02a41,0xb0182df3,0xb10f,0x30182df3,
0x2df3b018,0x2c983082,0xb10f8000,0x2c98b082,0xadf3b018,0x30d0aa41,0x310f,0xb0d0aa41,0xb0d02a41,0xa655b0ff,0x310f0000,0xa65530ff,
0x310f,0xb10f8000,0xb10f,0x310f0000,0x30d02a41,0xa65530ff,0xb10f8000,0xa655b0ff,0xb0d02a41,0x30d02a41,0xadf3b018,0x310f,
0x2df33018,0xa655b0ff,0xaa4130d0,0x2f28af28,0x2df3b018,0xb0182df3,0x30d0aa41,0xb10f8000,0xb0d0aa41,0x30822c98,0xb018adf3,0x2f282f28,
0x310f,0xb10f,0x310f,0xb10f,0x30d0aa41,0xb0822c98,0x3018adf3,0xaf282f28,0xadf3b018,0x30182df3,0xb0d0aa41,0x310f0000,
0xadf33018,0x2655b0ff,0x2a4130d0,0xaf28af28,0x30d02a41,0xb0d02a41,0x2df3b018,0x310f,0xb10f,0x2f282f28,0xb10f8000,0x2f28af28,
0xb0d02a41,0x2a41b0d0,0x2df33018,0xb10f8000,0x2df33018,0xb0ff2655,0x2a41b0d0,0x2f282f28,0x2df3b018,0x2df33018,0xb0d02a41,0xb10f,
0xb0d0aa41,0x2c98b082,0x30182df3,0xaf282f28,0x310f,0xb10f8000,0xb10f,0x310f0000,0x30d0aa41,0x2c983082,0xb0182df3,0xaf28af28,
0x310f8000,0xb018adf3,0x2a4130d0,0x2a41b0d0,0x310f8000,0xb082ac98,0x2df33018,0xa655b0ff,0x310f8000,0xb0d0aa41,0x30182df3,0xadf3b018,
0x310f8000,0xb0ffa655,0x30d02a41,0xb082ac98,0x310f8000,0xb10f8000,0x310f8000,0xb10f8000,0x310f8000,0xb0ff2655,0x30d0aa41,0xb0822c98,
0x310f8000,0xb0d02a41,0x3018adf3,0xadf33018,0x310f8000,0xb0822c98,0x2df3b018,0xa65530ff,0x310f8000,0xb0182df3,0x2a41b0d0,0x2a4130d0,
0x310f8000,0xaf282f28,0xb10f,0x2f282f28,0x310f8000,0xadf33018,0xaa41b0d0,0x30d02a41,0x310f8000,0xac983082,0xadf3b018,0x30ffa655,
0x310f8000,0xaa4130d0,0xb018adf3,0x3018adf3,0x310f8000,0xa65530ff,0xb0d0aa41,0x2c98b082,0x310f8000,0x8000310f,0xb10f8000,0xb10f,
0x310f8000,0x265530ff,0xb0d02a41,0xac98b082,0xb0182df3,0x310f8000,0xb018adf3,0x2a4130d0,0xaa4130d0,0x2f28af28,0xb0d02a41,0x30ff2655,
0x2a4130d0,0xb10f,0xaa4130d0,0x2df3b018,0x30182df3,0xaf28af28,0x2df33018,0xac98b082,0x310f8000,0xb10f8000,0x310f8000,0xb10f8000,
0x3018adf3,0xaf282f28,0x2df3b018,0xac983082,0x2a41b0d0,0x8000310f,0xaa41b0d0,0x2df33018,0xaa41b0d0,0x2f282f28,0xb0d0aa41,0x30ffa655,
0xb018adf3,0x310f8000,0xb0182df3,0x2a41b0d0,0xb10f8000,0x2f28af28,0x8000310f,0xaf28af28,0xb0182df3,0xb10f,0x30182df3,0xb0d02a41,
0xaa4130d0,0xaf28af28,0x30d0aa41,0xa65530ff,0x2a4130d0,0xb10f8000,0x2a41b0d0,0x30182df3,0x30182df3,0xaf282f28,0xadf3b018,0x3082ac98,
0x310f8000,0x8000310f,0xb10f8000,0xb10f,0x3018adf3,0x2f282f28,0xadf33018,0xb082ac98,0x2a41b0d0,0xb0182df3,0x310f8000,0xb018adf3,
0xb018adf3,0x2c983082,0xb10f,0xac983082,0xb0182df3,0x30d0aa41,0xb10f8000,0x30d02a41,0x2a4130d0,0xa655b0ff,0x8000310f,0x2655b0ff,
0x310f8000,0xb10f8000,0x310f8000,0xb10f8000,0x2a41b0d0,0xa65530ff,0xb10f,0x265530ff,0xb018adf3,0x30d02a41,0xb10f8000,0x30d0aa41,
0xb0182df3,0x2c98b082,0x8000310f,0xac98b082,0x2a4130d0,0xb018adf3,0x310f8000,0xb0182df3,0x310f8000,0xaf282f28,0xb10f,0x2f282f28,
0x2a41b0d0,0x2df33018,0xb10f8000,0x2df3b018,0xb018adf3,0x3082ac98,0x8000310f,0xb082ac98,0xb0182df3,0xaa41b0d0,0x310f8000,0xaa4130d0,
0x2a4130d0,0xb0ff2655,0xb10f,0x30ff2655,0x310f8000,0x8000310f,0xb10f8000,0xb10f,0x2a41b0d0,0x30ff2655,0x8000310f,0xb0ff2655,
0x2a4130d0,0x2a41b0d0,0xb0182df3,0x310f8000,0x3018adf3,0xb0ff2655,0x30d02a41,0xaf28af28,0xb018adf3,0x2df33018,0xaa41b0d0,0x8000310f,
0xaa4130d0,0x2c98b082,0xadf33018,0x2f28af28,0x310f8000,0xb10f8000,0x310f8000,0xb10f8000,0xaa41b0d0,0x2c983082,0xadf3b018,0x2f282f28,
0xb0182df3,0x2df3b018,0xaa4130d0,0xb10f,0x30182df3,0xb0ffa655,0x30d0aa41,0xaf282f28,0x2a41b0d0,0x2a4130d0,0xb018adf3,0x310f8000,
0xb10f8000,0x2f28af28,0x8000310f,0xaf28af28,0x2a4130d0,0xb0d0aa41,0x3018adf3,0x8000310f,0x3018adf3,0x265530ff,0xb0d0aa41,0x2f28af28,
0xb018adf3,0x3018adf3,0x2a4130d0,0xb10f8000,0xaa4130d0,0xb082ac98,0x2df3b018,0x2f282f28,0x310f8000,0x8000310f,0xb10f8000,0xb10f,
0xaa41b0d0,0x3082ac98,0x2df33018,0xaf282f28,0x2df3b018,0xb0d02a41,0x30d02a41,0xadf3b018,0xb0d02a41,0x30ff2655,0xb018adf3,0x2c983082,
0x30d02a41,0xb018adf3,0x2df33018,0xaa41b0d0,0xadf3b018,0x2c983082,0xaa41b0d0,0x265530ff,0x310f,0xb10f,0x310f,0xb10f,
0x2df3b018,0xac983082,0x2a41b0d0,0xa65530ff,0xb0d02a41,0x3018adf3,0xadf33018,0x2a41b0d0,0x30d02a41,0xb0ff2655,0x3018adf3,0xac983082,
0xadf3b018,0x30d02a41,0xb0d02a41,0x2df3b018,0x310f,0xaf28af28,0x310f0000,0xaf282f28,0x2df3b018,0x2a4130d0,0xb0d0aa41,0x3018adf3,
0xb0d02a41,0x2655b0ff,0x30182df3,0xb0822c98,0x30d02a41,0xadf33018,0xadf3b018,0x30d0aa41,0xadf3b018,0x3082ac98,0x2a4130d0,0xb0ff2655,
0x310f,0xb10f8000,0xb10f,0x310f0000,0x2df3b018,0x30822c98,0xaa4130d0,0xb0ffa655,0x310f,0x2df3b018,0xb0d02a41,0x30d02a41,
0xb10f,0xac983082,0x3018adf3,0xb0ff2655,0x310f,0x2a41b0d0,0xadf33018,0x3018adf3,0xb10f,0xa65530ff,0x2a41b0d0,0xac983082,
0x310f,0xb10f,0x310f,0xb10f,0xb10f,0x265530ff,0xaa41b0d0,0x2c983082,0x310f,0xaa41b0d0,0x2df33018,0xb018adf3,
0xb10f,0x2c983082,0xb018adf3,0x30ff2655,0x310f,0xadf3b018,0x30d02a41,0xb0d02a41,0xb10f,0x2f282f28,0xb10f8000,0x2f28af28,
0x310f,0xb018adf3,0x30d0aa41,0xaa4130d0,0xb10f,0x30822c98,0xb0182df3,0xa655b0ff,0x310f,0xb0d0aa41,0x2df3b018,0x2df33018,
0xb10f,0x30ff2655,0xaa4130d0,0xb082ac98,0x310f,0xb10f8000,0xb10f,0x310f0000,0xb10f,0x30ffa655,0x2a4130d0,0xb0822c98,
0xadf3b018,0x310f,0x2df3b018,0xb0d02a41,0x30d02a41,0xaf28af28,0x2a4130d0,0x2655b0ff,0xb0d02a41,0x310f0000,0xb0d0aa41,0x30182df3,
0x2df3b018,0xaf282f28,0x3018adf3,0xb0822c98,0x310f,0xb10f,0x310f,0xb10f,0xadf3b018,0x2f282f28,0xb018adf3,0x30822c98,
0x30d02a41,0xb10f8000,0x30d0aa41,0xb0182df3,0xb0d02a41,0x2f28af28,0xaa4130d0,0xa655b0ff,0x2df3b018,0x310f,0xadf3b018,0x30d02a41,
0x310f,0xaf28af28,0x310f0000,0xaf282f28,0xadf3b018,0x310f0000,0xadf33018,0xaa41b0d0,0x30d02a41,0xaf282f28,0xaa41b0d0,0x30ff2655,
0xb0d02a41,0xb10f,0x30d02a41,0xadf33018,0x2df3b018,0x2f282f28,0xb0182df3,0xac98b082,0x310f,0xb10f8000,0xb10f,0x310f0000,
0xadf3b018,0x2f28af28,0x30182df3,0xac983082,0x30d02a41,0xadf3b018,0x310f,0x2df3b018,0xadf33018,0x3082ac98,0xb10f8000,0x30822c98,
0xadf3b018,0x2a4130d0,0xb10f,0xaa4130d0,0x30d0aa41,0xb0ff2655,0x310f0000,0xb0ffa655,0x310f,0xb10f,0x310f,0xb10f,
0xb0d0aa41,0x30ff2655,0xb10f8000,0x30ffa655,0x2df3b018,0xaa4130d0,0xb10f,0x2a4130d0,0x2df33018,0xb082ac98,0x310f0000,0xb0822c98,
0xb0d02a41,0x2df3b018,0x310f,0xadf3b018,0xb10f,0x2f282f28,0xb10f8000,0x2f28af28,0x30d02a41,0xb0182df3,0xb10f,0x30182df3,
0xadf33018,0xac98b082,0x310f0000,0xac983082,0xadf3b018,0x30d0aa41,0x310f,0xb0d0aa41,0x30d0aa41,0x265530ff,0xb10f8000,0x2655b0ff,
0x310f,0xb10f8000,0xb10f,0x310f0000,0xb0d0aa41,0x2655b0ff,0x310f0000,0x265530ff,0xb018adf3,0x2a4130d0,0x2a41b0d0,0xb0182df3,
0x2a4130d0,0x2655b0ff,0xadf33018,0x3082ac98,0x2a41b0d0,0xadf33018,0x3018adf3,0xb0d02a41,0xb0182df3,0x3082ac98,0xb0d02a41,0x30ffa655,
0x310f8000,0xb10f8000,0x310f8000,0xb10f8000,0xb018adf3,0x30822c98,0xb0d0aa41,0x30ff2655,0x2a4130d0,0xadf3b018,0x30182df3,0xb0d0aa41,
0x2a41b0d0,0x265530ff,0xadf3b018,0x30822c98,0xb0182df3,0x2a41b0d0,0x2a4130d0,0xb018adf3,0x310f8000,0xaf282f28,0xb10f,0x2f282f28,
0xb018adf3,0x30d0aa41,0xaa4130d0,0xadf3b018,0x2a4130d0,0xb0ffa655,0x2df3b018,0x2c983082,0x2a41b0d0,0x30182df3,0xb0182df3,0xaa41b0d0,
0xb0182df3,0xac98b082,0x30d0aa41,0x265530ff,0x310f8000,0x8000310f,0xb10f8000,0xb10f,0xb018adf3,0x2c98b082,0x30d02a41,0xa65530ff,
0x310f8000,0xb018adf3,0x2a4130d0,0x2a41b0d0,0xb10f8000,0x30822c98,0xadf3b018,0x265530ff,0x310f8000,0xb0d0aa41,0x30182df3,0xadf3b018,
0xb10f8000,0x30ff2655,0xb0d0aa41,0x30822c98,0x310f8000,0xb10f8000,0x310f8000,0xb10f8000,0xb10f8000,0x30ffa655,0xb0d02a41,0x3082ac98,
0x310f8000,0xb0d02a41,0x3018adf3,0xadf33018,0xb10f8000,0x3082ac98,0xadf33018,0x2655b0ff,0x310f8000,0xb0182df3,0x2a41b0d0,0x2a4130d0,
0xb10f8000,0x2f28af28,0x8000310f,0xaf28af28,0x310f8000,0xadf33018,0xaa41b0d0,0x30d02a41,0xb10f8000,0x2c98b082,0x2df33018,0xb0ff2655,
0x310f8000,0xaa4130d0,0xb018adf3,0x3018adf3,0xb10f8000,0x2655b0ff,0x30d02a41,0xac983082,0x310f8000,0x8000310f,0xb10f8000,0xb10f,
0xb10f8000,0xa655b0ff,0x30d0aa41,0x2c983082,0xb0182df3,0x310f8000,0xb018adf3,0x2a4130d0,0x2a41b0d0,0xaf282f28,0x30d0aa41,0xb0ffa655,
0x2a4130d0,0xb10f,0xaa4130d0,0x2df3b018,0xb018adf3,0x2f282f28,0xadf3b018,0x2c983082,0x310f8000,0xb10f8000,0x310f8000,0xb10f8000,
0xb0182df3,0x2f28af28,0xadf33018,0x2c98b082,0x2a41b0d0,0x8000310f,0xaa41b0d0,0x2df33018,0x2a4130d0,0xaf28af28,0x30d02a41,0xb0ff2655,
0xb018adf3,0x310f8000,0xb0182df3,0x2a41b0d0,0x310f8000,0xaf282f28,0xb10f,0x2f282f28,0xb0182df3,0xb10f,0x30182df3,0xb0d02a41,
0x2a41b0d0,0x2f282f28,0xb0d02a41,0x2655b0ff,0x2a4130d0,0xb10f8000,0x2a41b0d0,0x30182df3,0xb018adf3,0x2f28af28,0x2df33018,0xb0822c98,
0x310f8000,0x8000310f,0xb10f8000,0xb10f,0xb0182df3,0xaf28af28,0x2df3b018,0x30822c98,0x2a41b0d0,0xb0182df3,0x310f8000,0xb018adf3,
0x30182df3,0xac98b082,0x8000310f,0x2c98b082,0xb0182df3,0x30d0aa41,0xb10f8000,0x30d02a41,0xaa41b0d0,0x265530ff,0xb10f,0xa65530ff,
0x310f8000,0xb10f8000,0x310f8000,0xb10f8000,0xaa4130d0,0x2655b0ff,0x8000310f,0xa655b0ff,0xb018adf3,0x30d02a41,0xb10f8000,0x30d0aa41,
0x3018adf3,0xac983082,0xb10f,0x2c983082,0x2a4130d0,0xb018adf3,0x310f8000,0xb0182df3,0xb10f8000,0x2f28af28,0x8000310f,0xaf28af28,
0x2a41b0d0,0x2df33018,0xb10f8000,0x2df3b018,0x30182df3,0xb0822c98,0xb10f,0x30822c98,0xb0182df3,0xaa41b0d0,0x310f8000,0xaa4130d0,
0xaa41b0d0,0x30ffa655,0x8000310f,0xb0ffa655,0x310f8000,0x8000310f,0xb10f8000,0xb10f,0xaa4130d0,0xb0ffa655,0xb10f,0x30ffa655,
0xb0d02a41,0x30d02a41,0xadf3b018,0x310f,0xadf3b018,0x265530ff,0x2a41b0d0,0xaf282f28,0x2df3b018,0xb0182df3,0x30d0aa41,0xb10f8000,
0x30d02a41,0xb082ac98,0x30182df3,0xaf28af28,0x310f,0xb10f,0x310f,0xb10f,0xb0d02a41,0x3082ac98,0xb0182df3,0x2f28af28,
0xadf3b018,0x30182df3,0xb0d0aa41,0x310f0000,0x2df3b018,0xa65530ff,0xaa41b0d0,0x2f282f28,0x30d02a41,0xb0d02a41,0x2df3b018,0x310f,
0x310f,0xaf28af28,0x310f0000,0xaf282f28,0xb0d02a41,0x2a41b0d0,0x2df33018,0xb10f8000,0xadf3b018,0x30ffa655,0xaa4130d0,0xaf28af28,
0x2df3b018,0x2df33018,0xb0d02a41,0xb10f,0x30d02a41,0xac983082,0xb018adf3,0x2f28af28,0x310f,0xb10f8000,0xb10f,0x310f0000,
0xb0d02a41,0xac98b082,0x3018adf3,0x2f282f28,0x2df3b018,0xb0d02a41,0x30d02a41,0xadf3b018,0x30d0aa41,0xb0ffa655,0x30182df3,0xac98b082,
0x30d02a41,0xb018adf3,0x2df33018,0xaa41b0d0,0x2df33018,0xac98b082,0x2a4130d0,0xa655b0ff,0x310f,0xb10f,0x310f,0xb10f,
0xadf33018,0x2c98b082,0xaa4130d0,0x2655b0ff,0xb0d02a41,0x3018adf3,0xadf33018,0x2a41b0d0,0xb0d0aa41,0x30ffa655,0xb0182df3,0x2c98b082,
0xadf3b018,0x30d02a41,0xb0d02a41,0x2df3b018,0xb10f,0x2f282f28,0xb10f8000,0x2f28af28,0x2df3b018,0x2a4130d0,0xb0d0aa41,0x3018adf3,
0x30d0aa41,0xa65530ff,0xb018adf3,0x3082ac98,0x30d02a41,0xadf33018,0xadf3b018,0x30d0aa41,0x2df33018,0xb0822c98,0xaa41b0d0,0x30ffa655,
0x310f,0xb10f8000,0xb10f,0x310f0000,0xadf33018,0xb082ac98,0x2a41b0d0,0x30ff2655,0,0,0,0,
0,0,0,0,0,0,0,0,0,0,0,0,
0,0,0,0,0,0,0,0,0,0,0,0,
0,0,0,0,0,0,0,0,0,0,0,0,
0,0,0,0,0,0,0,0,0,0,0,0,
0,0,0,0,0,0,0,0,0,0,0,0,
0,0,0,0,0,0,0,0,0,0,0,0,
0,0,0,0,0,0,0,0,0,0,0,0,
0,0,0,0,0,0,0,0,0,0,0,0,
0,0,0,0,0,0,0,0,0,0,0,0,
0,0,0,0,0,0,0,0,0,0,0,0,
0,0,0,0,0x2a4130d0,0x2a41b0d0,0xb0182df3,0x310f8000,0xb0182df3,0x30ffa655,0xb0d0aa41,0x2f282f28,
0xb018adf3,0x2df33018,0xaa41b0d0,0x8000310f,0x2a41b0d0,0xac983082,0x2df3b018,0xaf282f28,0x310f8000,0xb10f8000,0x310f8000,0xb10f8000,
0x2a4130d0,0xac98b082,0x2df33018,0xaf28af28,0xb0182df3,0x2df3b018,0xaa4130d0,0xb10f,0xb018adf3,0x30ff2655,0xb0d02a41,0x2f28af28,
0x2a41b0d0,0x2a4130d0,0xb018adf3,0x310f8000,0x310f8000,0xaf282f28,0xb10f,0x2f282f28,0x2a4130d0,0xb0d0aa41,0x3018adf3,0x8000310f,
0xb0182df3,0xa655b0ff,0x30d02a41,0xaf282f28,0xb018adf3,0x3018adf3,0x2a4130d0,0xb10f8000,0x2a41b0d0,0x30822c98,0xadf33018,0xaf28af28,
0x310f8000,0x8000310f,0xb10f8000,0xb10f,0x2a4130d0,0xb0822c98,0xadf3b018,0x2f28af28,0xb018adf3,0x2a4130d0,0x2a41b0d0,0xb0182df3,
0xaa41b0d0,0xa65530ff,0x2df3b018,0xb0822c98,0x2a41b0d0,0xadf33018,0x3018adf3,0xb0d02a41,0x3018adf3,0xb0822c98,0x30d0aa41,0xb0ff2655,
0x310f8000,0xb10f8000,0x310f8000,0xb10f8000,0x30182df3,0xb082ac98,0x30d02a41,0xb0ffa655,0x2a4130d0,0xadf3b018,0x30182df3,0xb0d0aa41,
0xaa4130d0,0xa655b0ff,0x2df33018,0xb082ac98,0xb0182df3,0x2a41b0d0,0x2a4130d0,0xb018adf3,0xb10f8000,0x2f28af28,0x8000310f,0xaf28af28,
0xb018adf3,0x30d0aa41,0xaa4130d0,0xadf3b018,0xaa41b0d0,0x30ff2655,0xadf33018,0xac98b082,0x2a41b0d0,0x30182df3,0xb0182df3,0xaa41b0d0,
0x3018adf3,0x2c983082,0xb0d02a41,0xa655b0ff,0x310f8000,0x8000310f,0xb10f8000,0xb10f,0x30182df3,0xac983082,0xb0d0aa41,0x2655b0ff,
0,0,0,0,0,0,0,0,0,0,0,0,
0,0,0,0,0,0,0,0,0,0,0,0,
0,0,0,0,0,0,0,0,0,0,0,0,
0,0,0,0,0,0,0,0,0,0,0,0,
0,0,0,0,0,0,0,0,0,0,0,0,
0,0,0,0,0,0,0,0,0,0,0,0,
0,0,0,0,0,0,0,0,0,0,0,0,
0,0,0,0,0,0,0,0,0,0,0,0,
0,0,0,0,0,0,0,0,0,0,0,0,
0,0,0,0,0,0,0,0,0,0,0,0,
0,0,0,0,0,0,0,0,0x310f,0xb0d02a41,0xadf3b018,0x2df3b018,
0x310f,0xb0822c98,0xb018adf3,0x2655b0ff,0x310f,0xb0182df3,0xb0d0aa41,0xaa41b0d0,0x310f,0xaf282f28,0xb10f8000,0xaf28af28,
0x310f,0xadf33018,0xb0d02a41,0xb0d0aa41,0x310f,0xac983082,0xb0182df3,0xb0ff2655,0x310f,0xaa4130d0,0xadf33018,0xb0182df3,
0x310f,0xa65530ff,0xaa4130d0,0xac983082,0,0,0,0,0,0,0,0,
0,0,0,0,0,0,0,0,0,0,0,0,
0,0,0,0,0,0,0,0,0,0,0,0,
0x30d02a41,0x310f,0xb0d02a41,0xadf3b018,0x30d0aa41,0x2f282f28,0xaa4130d0,0xb0ff2655,0x2df3b018,0x310f0000,0x2df33018,0xaa4130d0,
0xb10f,0x2f28af28,0x310f0000,0x2f282f28,0xadf3b018,0xb10f,0x2df3b018,0x30d0aa41,0xb0d0aa41,0xaf28af28,0xaa41b0d0,0x2655b0ff,
0xb0d02a41,0xb10f8000,0xb0d0aa41,0xb018adf3,0xadf33018,0xaf282f28,0xb0182df3,0xb0822c98,0,0,0,0,
0,0,0,0,0,0,0,0,0,0,0,0,
0,0,0,0,0,0,0,0,0,0,0,0,
0,0,0,0,0x2df3b018,0x30d02a41,0x310f,0xb0d02a41,0xadf3b018,0x2c98b082,0x310f0000,0x2c983082,
0xb0d02a41,0xb018adf3,0xb10f,0x3018adf3,0x310f,0xaf282f28,0xb10f8000,0xaf28af28,0x30d02a41,0x2df33018,0x310f,0xadf33018,
0x2df3b018,0x3082ac98,0x310f0000,0x30822c98,0xadf3b018,0xaa41b0d0,0xb10f,0x2a41b0d0,0xb0d02a41,0xb0ff2655,0xb10f8000,0xb0ffa655,
0,0,0,0,0,0,0,0,0,0,0,0,
0,0,0,0,0,0,0,0,0,0,0,0,
0,0,0,0,0,0,0,0,0xadf3b018,0x2df3b018,0x30d02a41,0x310f,
0xadf33018,0xb0ffa655,0xaa41b0d0,0x2f28af28,0x30d02a41,0x2a4130d0,0xadf33018,0xb10f8000,0xb10f,0x2f28af28,0x310f0000,0x2f282f28,
0xb0d02a41,0xb0d0aa41,0xadf3b018,0xb10f,0x2df33018,0x265530ff,0xaa4130d0,0xaf282f28,0x2df3b018,0x3018adf3,0x30d0aa41,0x310f0000,
0xb0d0aa41,0xb082ac98,0xb018adf3,0xaf28af28,0,0,0,0,0,0,0,0,
0,0,0,0,0,0,0,0,0,0,0,0,
0,0,0,0,0,0,0,0,0,0,0,0,
0x310f8000,0x2a4130d0,0xb0182df3,0xb018adf3,0x310f8000,0x2c983082,0xadf33018,0xb0ffa655,0x310f8000,0x2df33018,0xaa4130d0,0xb0d02a41,
0x310f8000,0x2f282f28,0x8000310f,0xaf282f28,0x310f8000,0x30182df3,0x2a4130d0,0xaa4130d0,0x310f8000,0x30822c98,0x2df33018,0x265530ff,
0x310f8000,0x30d02a41,0x30182df3,0x2df33018,0x310f8000,0x30ff2655,0x30d02a41,0x30822c98,0,0,0,0,
0,0,0,0,0,0,0,0,0,0,0,0,
0,0,0,0,0,0,0,0,0,0,0,0,
0,0,0,0,0x2a41b0d0,0x310f8000,0x2a4130d0,0xb0182df3,0xaa41b0d0,0x2f28af28,0x30d02a41,0x265530ff,
0xb018adf3,0xb10f,0x3018adf3,0x30d02a41,0xb10f8000,0xaf28af28,0xb10f,0x2f28af28,0xb0182df3,0xb10f8000,0xb018adf3,0xaa41b0d0,
0xaa4130d0,0xaf282f28,0xb0d02a41,0xb0ffa655,0x2a4130d0,0x8000310f,0xaa4130d0,0xadf33018,0x30182df3,0x2f282f28,0x2df33018,0x2c983082,
0,0,0,0,0,0,0,0,0,0,0,0,
0,0,0,0,0,0,0,0,0,0,0,0,
0,0,0,0,0,0,0,0,0xb018adf3,0x2a41b0d0,0x310f8000,0x2a4130d0,
0xb0182df3,0xb082ac98,0xb10f,0x3082ac98,0x2a4130d0,0xadf33018,0xb10f8000,0xadf3b018,0x310f8000,0x2f282f28,0x8000310f,0xaf282f28,
0x2a41b0d0,0x3018adf3,0x310f8000,0x30182df3,0xb018adf3,0xac98b082,0xb10f,0x2c98b082,0xb0182df3,0xb0d02a41,0xb10f8000,0xb0d0aa41,
0x2a4130d0,0x265530ff,0x8000310f,0xa65530ff,0,0,0,0,0,0,0,0,
0,0,0,0,0,0,0,0,0,0,0,0,
0,0,0,0,0,0,0,0,0,0,0,0,
0xb0182df3,0xb018adf3,0x2a41b0d0,0x310f8000,0x30182df3,0xa65530ff,0xb0d02a41,0xaf28af28,0x2a41b0d0,0x30d0aa41,0x30182df3,0x8000310f,
0xb10f8000,0xaf28af28,0xb10f,0x2f28af28,0x2a4130d0,0xaa4130d0,0xb0182df3,0xb10f8000,0x3018adf3,0x30ffa655,0x30d02a41,0x2f282f28,
0xb018adf3,0xadf3b018,0xaa41b0d0,0xb10f,0xaa4130d0,0xac983082,0xadf33018,0xaf282f28,0,0,0,0,
0,0,0,0,0,0,0,0,0,0,0,0,
0,0,0,0,0,0,0,0,0,0,0,0,
0,0,0,0,0xb0d02a41,0xadf3b018,0x2df3b018,0x30d02a41,0x30d02a41,0x265530ff,0xb0182df3,0xb082ac98,
0xadf3b018,0x2a41b0d0,0x30d0aa41,0x30182df3,0x310f,0xaf282f28,0xb10f8000,0xaf28af28,0x2df3b018,0x30d0aa41,0x30d02a41,0x2df33018,
0xb0d02a41,0xb0ffa655,0xb018adf3,0xac98b082,0x30d02a41,0x30182df3,0x2df33018,0x2a4130d0,0xadf3b018,0xac98b082,0xaa41b0d0,0xa655b0ff,
0,0,0,0,0,0,0,0,0,0,0,0,
0,0,0,0,0,0,0,0,0,0,0,0,
0,0,0,0,0,0,0,0,0x310f,0xb0d02a41,0xadf3b018,0x2df3b018,
0xb10f,0x3082ac98,0x30182df3,0xa65530ff,0x310f,0xb0182df3,0xb0d0aa41,0xaa41b0d0,0xb10f,0x2f28af28,0x310f0000,0x2f282f28,
0x310f,0xadf33018,0xb0d02a41,0xb0d0aa41,0xb10f,0x2c98b082,0x3018adf3,0x30ffa655,0x310f,0xaa4130d0,0xadf33018,0xb0182df3,
0xb10f,0x2655b0ff,0x2a41b0d0,0x2c98b082,0,0,0,0,0,0,0,0,
0,0,0,0,0,0,0,0,0,0,0,0,
0,0,0,0,0,0,0,0,0,0,0,0,
0x30d02a41,0x310f,0xb0d02a41,0xadf3b018,0xb0d02a41,0xaf28af28,0x2a41b0d0,0x30ffa655,0x2df3b018,0x310f0000,0x2df33018,0xaa4130d0,
0x310f,0xaf282f28,0xb10f8000,0xaf28af28,0xadf3b018,0xb10f,0x2df3b018,0x30d0aa41,0x30d02a41,0x2f282f28,0x2a4130d0,0xa65530ff,
0xb0d02a41,0xb10f8000,0xb0d0aa41,0xb018adf3,0x2df3b018,0x2f28af28,0x3018adf3,0x3082ac98,0,0,0,0,
0,0,0,0,0,0,0,0,0,0,0,0,
0,0,0,0,0,0,0,0,0,0,0,0,
0,0,0,0,0x2df3b018,0x30d02a41,0x310f,0xb0d02a41,0x2df33018,0xac983082,0xb10f8000,0xac98b082,
0xb0d02a41,0xb018adf3,0xb10f,0x3018adf3,0xb10f,0x2f28af28,0x310f0000,0x2f282f28,0x30d02a41,0x2df33018,0x310f,0xadf33018,
0xadf33018,0xb0822c98,0xb10f8000,0xb082ac98,0xadf3b018,0xaa41b0d0,0xb10f,0x2a41b0d0,0x30d0aa41,0x30ffa655,0x310f0000,0x30ff2655,
0,0,0,0,0,0,0,0,0,0,0,0,
0,0,0,0,0,0,0,0,0,0,0,0,
0,0,0,0,0,0,0,0,0x2a4130d0,0xb0182df3,0xb018adf3,0x2a41b0d0,
0x2a41b0d0,0x30ffa655,0x2df33018,0xac983082,0xb0182df3,0xb0d0aa41,0xaa41b0d0,0x2df3b018,0x310f8000,0x2f282f28,0x8000310f,0xaf282f28,
0xb018adf3,0xaa41b0d0,0x2a41b0d0,0x3018adf3,0x2a4130d0,0xa65530ff,0xadf33018,0xb0822c98,0x2a41b0d0,0x2df3b018,0x3018adf3,0x30d0aa41,
0xb0182df3,0xb0822c98,0xb0d02a41,0xb0ff2655,0,0,0,0,0,0,0,0,
0,0,0,0,0,0,0,0,0,0,0,0,
0,0,0,0,0,0,0,0,0,0,0,0,
0x310f8000,0x2a4130d0,0xb0182df3,0xb018adf3,0xb10f8000,0xac98b082,0x2df3b018,0x30ff2655,0x310f8000,0x2df33018,0xaa4130d0,0xb0d02a41,
0xb10f8000,0xaf28af28,0xb10f,0x2f28af28,0x310f8000,0x30182df3,0x2a4130d0,0xaa4130d0,0xb10f8000,0xb082ac98,0xadf3b018,0xa655b0ff,
0x310f8000,0x30d02a41,0x30182df3,0x2df33018,0xb10f8000,0xb0ffa655,0xb0d0aa41,0xb082ac98,0,0,0,0,
0,0,0,0,0,0,0,0,0,0,0,0,
0,0,0,0,0,0,0,0,0,0,0,0,
0,0,0,0,0x2a41b0d0,0x310f8000,0x2a4130d0,0xb0182df3,0x2a4130d0,0xaf282f28,0xb0d0aa41,0xa655b0ff,
0xb018adf3,0xb10f,0x3018adf3,0x30d02a41,0x310f8000,0x2f282f28,0x8000310f,0xaf282f28,0xb0182df3,0xb10f8000,0xb018adf3,0xaa41b0d0,
0x2a41b0d0,0x2f28af28,0x30d0aa41,0x30ff2655,0x2a4130d0,0x8000310f,0xaa4130d0,0xadf33018,0xb018adf3,0xaf28af28,0xadf3b018,0xac98b082,
0,0,0,0,0,0,0,0,0,0,0,0,
0,0,0,0,0,0,0,0,0,0,0,0,
0,0,0,0,0,0,0,0,0xb018adf3,0x2a41b0d0,0x310f8000,0x2a4130d0,
0x3018adf3,0x30822c98,0x8000310f,0xb0822c98,0x2a4130d0,0xadf33018,0xb10f8000,0xadf3b018,0xb10f8000,0xaf28af28,0xb10f,0x2f28af28,
0x2a41b0d0,0x3018adf3,0x310f8000,0x30182df3,0x30182df3,0x2c983082,0x8000310f,0xac983082,0xb0182df3,0xb0d02a41,0xb10f8000,0xb0d0aa41,
0xaa41b0d0,0xa655b0ff,0xb10f,0x2655b0ff,0,0,0,0,0,0,0,0,
0,0,0,0,0,0,0,0,0,0,0,0,
0,0,0,0,0,0,0,0,0,0,0,0,
0xadf3b018,0x2df3b018,0x30d02a41,0x310f,0x2df3b018,0x30ff2655,0x2a4130d0,0xaf282f28,0x30d02a41,0x2a4130d0,0xadf33018,0xb10f8000,
0x310f,0xaf282f28,0xb10f8000,0xaf28af28,0xb0d02a41,0xb0d0aa41,0xadf3b018,0xb10f,0xadf3b018,0xa655b0ff,0x2a41b0d0,0x2f28af28,
0x2df3b018,0x3018adf3,0x30d0aa41,0x310f0000,0x30d02a41,0x30822c98,0x30182df3,0x2f282f28,0,0,0,0,
0,0,0,0,0,0,0,0,0,0,0,0,
0,0,0,0,0,0,0,0,0,0,0,0,
0,0,0,0,0xb0d02a41,0xadf3b018,0x2df3b018,0x30d02a41,0xb0d0aa41,0xa655b0ff,0x3018adf3,0x30822c98,
0xadf3b018,0x2a41b0d0,0x30d0aa41,0x30182df3,0xb10f,0x2f28af28,0x310f0000,0x2f282f28,0x2df3b018,0x30d0aa41,0x30d02a41,0x2df33018,
0x30d0aa41,0x30ff2655,0x30182df3,0x2c983082,0x30d02a41,0x30182df3,0x2df33018,0x2a4130d0,0x2df33018,0x2c983082,0x2a4130d0,0x265530ff,
0,0,0,0,0,0,0,0,0,0,0,0,
0,0,0,0,0,0,0,0,0,0,0,0,
0,0,0,0,0,0,0,0,0,0,0,0,
0,0,0,0,0,0,0,0,0,0,0,0,
0,0,0,0,0,0,0,0,0,0,0,0,
0,0,0,0,0,0,0,0,0,0,0,0,
0,0,0,0,0,0,0,0,0,0,0,0,
0,0,0,0,0,0,0,0,0,0,0,0,
0,0,0,0,0,0,0,0,0,0,0,0,
0,0,0,0,0,0,0,0,0,0,0,0,
0,0,0,0,0,0,0,0,0,0,0,0,
0,0,0,0,0,0,0,0,0,0,0,0,
0,0,0,0,0,0,0,0,0,0,0,0,
0,0,0,0,0xb0182df3,0xb018adf3,0x2a41b0d0,0x310f8000,0xb018adf3,0x2655b0ff,0x30d0aa41,0x2f282f28,
0x2a41b0d0,0x30d0aa41,0x30182df3,0x8000310f,0x310f8000,0x2f282f28,0x8000310f,0xaf282f28,0x2a4130d0,0xaa4130d0,0xb0182df3,0xb10f8000,
0xb0182df3,0xb0ff2655,0xb0d0aa41,0xaf28af28,0xb018adf3,0xadf3b018,0xaa41b0d0,0xb10f,0x2a41b0d0,0x2c98b082,0x2df3b018,0x2f28af28,
0,0,0,0,0,0,0,0,0,0,0,0,
0,0,0,0,0,0,0,0,0,0,0,0,
0,0,0,0,0,0,0,0,0x2a4130d0,0xb0182df3,0xb018adf3,0x2a41b0d0,
0xaa4130d0,0xb0ff2655,0xadf3b018,0x2c98b082,0xb0182df3,0xb0d0aa41,0xaa41b0d0,0x2df3b018,0xb10f8000,0xaf28af28,0xb10f,0x2f28af28,
0xb018adf3,0xaa41b0d0,0x2a41b0d0,0x3018adf3,0xaa41b0d0,0x2655b0ff,0x2df3b018,0x3082ac98,0x2a41b0d0,0x2df3b018,0x3018adf3,0x30d0aa41,
0x3018adf3,0x3082ac98,0x30d0aa41,0x30ffa655,0,0,0,0,0,0,0,0,
0,0,0,0,0,0,0,0,0,0,0,0,
0,0,0,0,0,0,0,0,0,0,0,0,
0,0,0,0,0,0,0,0,0,0,0,0,
0,0,0,0,0,0,0,0,0,0,0,0,
0,0,0,0,0,0,0,0,0,0,0,0,
0,0,0,0,0,0,0,0,0,0,0,0,
0,0,0,0,0,0,0,0,0,0,0,0,
0,0,0,0,0,0,0,0,0,0,0,0,
0,0,0,0,0,0,0,0,0,0,0,0,
0,0,0,0,0,0,0,0,0,0,0,0,
0,0,0,0,0,0,0,0,0,0,0,0,
0,0,0,0,0,0,0,0,0,0,0,0,
0,0,0,0,0,0,0,0,
};

#define KMASK 63u
#define FPRIO 3
#define GPRIO 1
#define LPRIO 2
#define LSLP 0
#define A_Y 0u
#define A_W2 0u
#define A_H 0u
#define FPC 2
#define ROT2 0
#define HSLP 2
struct Params { const u16* x; const u16* wr; const u16* wi; const u16* w1; const u16* b1; const u16* w2; const u16* b2; u16* out; char* ws; u32 mask; u32 pad; };

__shared__ __attribute__((aligned(1024))) char smem[163840];
template <class T> DEV LAS T* lp(u32 off) { return (LAS T*)(smem + off); }
DEV u32 lds_base() { return (u32)(uintptr_t)(LAS char*)smem; }

DEV u4 mk_srd(const void* p, u32 bytes) { u64 a = (u64)p; return u4{(u32)a, (u32)(a >> 32) & 0xffffu, bytes, 0x00020000u}; }
#define RSRC(p, bytes) __builtin_amdgcn_make_buffer_rsrc((void*)(p), 0, (bytes), 0x00020000)
#define WAITV(n) asm volatile("s_waitcnt vmcnt(" #n ")" ::: "memory")

#define ACC(i, stmt) stmt
#define STAMP(i)
#define STDECL
#define STDUMP(wv)
DEV void arrive(u32 c) {
  u32 one = 1, addr = lds_base() + c;
  asm volatile("s_mov_b64 exec, 1\n\tds_add_u32 %0, %1\n\ts_mov_b64 exec, -1" :: "v"(addr), "v"(one) : "memory");
}
DEV void lwait(u32 c, u32 target) {
  u32 addr = lds_base() + c, v, sv;
  asm volatile("1:\n\tds_read_b32 %0, %2\n\ts_waitcnt lgkmcnt(0)\n\tv_readfirstlane_b32 %1, %0\n\ts_cmp_ge_u32 %1, %3\n\ts_cbranch_scc1 2f\n\t"
               "s_branch 1b\n2:" : "=&v"(v), "=&s"(sv) : "v"(addr), "s"(target) : "memory", "scc");
}
DEV void lwait_slow(u32 c, u32 target) {
  u32 addr = lds_base() + c, v, sv;
  asm volatile("1:\n\tds_read_b32 %0, %2\n\ts_waitcnt lgkmcnt(0)\n\tv_readfirstlane_b32 %1, %0\n\ts_cmp_ge_u32 %1, %3\n\ts_cbranch_scc1 2f\n\t"
               "s_sleep 2\n\ts_branch 1b\n2:" : "=&v"(v), "=&s"(sv) : "v"(addr), "s"(target) : "memory", "scc");
}
template <int LM> DEV void arrive_pub(u32 c, u32 last, u64 tag, u32 off, u4 srd) {
  u32 one = 1, addr = lds_base() + c, r, sv; u2 d = {(u32)tag, (u32)(tag >> 32)};
  asm volatile("s_mov_b64 exec, 1\n\tds_add_rtn_u32 %0, %2, %3\n\ts_waitcnt lgkmcnt(0)\n\tv_readfirstlane_b32 %1, %0\n\ts_cmp_eq_u32 %1, %4\n\ts_cbranch_scc0 1f\n\t"
               "s_mov_b64 exec, %8\n\tbuffer_store_dwordx2 %5, %6, %7, 0 offen sc1\n1:\n\ts_mov_b64 exec, -1"
               : "=&v"(r), "=&s"(sv) : "v"(addr), "v"(one), "s"(last), "v"(d), "v"(off), "s"(srd), "i"(LM) : "memory", "scc");
}
DEV void post(u32 c, int w, u32 v) {
  u32 addr = lds_base() + c + w * 4;
  asm volatile("ds_write_b32 %0, %1" :: "v"(addr), "v"(v) : "memory");
}
template <int SLP> DEV void wait4(u32 addr  , u32 target) {
  u32 v;
  asm volatile("1:\n\tds_read_b32 %0, %1\n\ts_waitcnt lgkmcnt(0)\n\tv_cmp_gt_u32 vcc, %2, %0\n\ts_nop 1\n\ts_cbranch_vccz 2f\n\ts_sleep %3\n\ts_branch 1b\n2:"
               : "=&v"(v) : "v"(addr), "s"(target), "i"(SLP) : "memory", "vcc");
}
template <int SC1> DEV void glds(u32 voff, u4 srd, u32 sbase, u32 si, u32 lbase, u32 li) {
  u32 t;
  if (SC1) asm volatile("s_add_u32 m0, %5, %6\n\ts_add_u32 %0, %3, %4\n\tbuffer_load_dwordx4 %1, %2, %0 offen sc1 lds" : "=&s"(t) : "v"(voff), "s"(srd), "s"(sbase), "i"(si), "s"(lbase), "i"(li) : "memory", "scc");
  else     asm volatile("s_add_u32 m0, %5, %6\n\ts_add_u32 %0, %3, %4\n\tbuffer_load_dwordx4 %1, %2, %0 offen lds" : "=&s"(t) : "v"(voff), "s"(srd), "s"(sbase), "i"(si), "s"(lbase), "i"(li) : "memory", "scc");
}
#define PIN(...) asm volatile("" : __VA_ARGS__)
#define SB() __builtin_amdgcn_sched_barrier(0)
constexpr u32 OOB = 0x80000000u;
DEV void st16_sc1(u4 v, u32 voff, u4 srd) {
  asm volatile("buffer_store_dwordx4 %0, %1, %2, 0 offen sc1\n\ts_nop 1" :: "v"(v), "v"(voff), "s"(srd) : "memory");
}
DEV void st16_sc1s(u4 v, u32 voff, u4 srd, u32 soff) {
  asm volatile("buffer_store_dwordx4 %0, %1, %2, %3 offen sc1\n\ts_nop 1" :: "v"(v), "v"(voff), "s"(srd), "s"(soff) : "memory");
}
DEV void st8_sc1_lane0(u64 v, u32 off, u4 srd) {
  u2 d = {(u32)v, (u32)(v >> 32)};
  asm volatile("s_mov_b64 exec, 1\n\tbuffer_store_dwordx2 %0, %1, %2, 0 offen sc1\n\ts_mov_b64 exec, -1" :: "v"(d), "v"(off), "s"(srd) : "memory");
}
DEV u32 pk_h2(float a, float b) { h2 v = {(_Float16)a, (_Float16)b}; return __builtin_bit_cast(u32, v); }
DEV u32 pk_b2(float a, float b) { b2 v = {(__bf16)a, (__bf16)b}; return __builtin_bit_cast(u32, v); }
DEV u2 trh(u32 addr) { return __builtin_bit_cast(u2, __builtin_amdgcn_ds_read_tr16_b64_v4i16(lp<s4>(addr))); }
DEV h2 ash2(u32 v) { return __builtin_bit_cast(h2, v); }
DEV u32 asu(h2 v) { return __builtin_bit_cast(u32, v); }
DEV float bflo(u32 v) { return __uint_as_float(v << 16); }
DEV float bfhi(u32 v) { return __uint_as_float(v & 0xffff0000u); }
DEV float rbf(float f) { return bflo(pk_b2(f, f) & 0xffffu); }
DEV f4 mfh(u4 a, u4 b, f4 c) { return __builtin_amdgcn_mfma_f32_16x16x32_f16(__builtin_bit_cast(h8, a), __builtin_bit_cast(h8, b), c, 0, 0, 0); }
DEV f4 mfb(u4 a, u4 b, f4 c) { return __builtin_amdgcn_mfma_f32_16x16x32_bf16(__builtin_bit_cast(b8, a), __builtin_bit_cast(b8, b), c, 0, 0, 0); }
DEV u4 twid_pack(f4 re, f4 im, f4 c, f4 s) {
  f4 r = re * c - im * s, i = re * s + im * c;
  return u4{pk_h2(r.x, i.x), pk_h2(r.y, i.y), pk_h2(r.z, i.z), pk_h2(r.w, i.w)};
}
DEV u4 pack4(f4 re, f4 im) { return u4{pk_h2(re.x, im.x), pk_h2(re.y, im.y), pk_h2(re.z, im.z), pk_h2(re.w, im.w)}; }
template <int C> DEV float dpp_ror_add(float v) { return v + __uint_as_float(__builtin_amdgcn_mov_dpp(__float_as_uint(v), 0x120 + C, 0xf, 0xf, true)); }
DEV float dot2b(u32 a, u32 b, float c) { return __builtin_amdgcn_fdot2_f32_bf16(__builtin_bit_cast(b2, a), __builtin_bit_cast(b2, b), c, false); }
template <int SLP> DEV void poll_one(__amdgpu_buffer_rsrc_t rs, u32 off, u64 tag) {
  _Pragma("nounroll") for (int sp = 0; sp < SPIN_MAX; ++sp) {
    u2 v = __builtin_amdgcn_raw_buffer_load_b64(rs, off, 0, 16);
    if (__all(((v.x ^ (u32)tag) | (v.y ^ (u32)(tag >> 32))) == 0)) break;
    __builtin_amdgcn_s_sleep(SLP);
  }
  asm volatile("" ::: "memory");
}
DEV void gather4(__amdgpu_buffer_rsrc_t rs, u32 off, u32 step, u32 t32, bool check, u4 (&v)[4]) {
  _Pragma("nounroll") for (int sp = 0; sp < SPIN_MAX; ++sp) {
#pragma unroll
    for (int n = 0; n < 4; ++n) v[n] = __builtin_amdgcn_raw_buffer_load_b128(rs, off, n * step, 16);
    if (!check || __all(((v[0].w ^ t32) | (v[1].w ^ t32) | (v[2].w ^ t32) | (v[3].w ^ t32)) == 0)) break;
    __builtin_amdgcn_s_sleep(3);
  }
}

DEV void f_path(const Params& P, int fw, int lane, int xl, int il) {
  int g = lane >> 4, l15 = lane & 15, ft = fw * 64 + lane;
  const int a = xl * 32 + il;
  const u32 mask = KMASK;
  const u4 sx = mk_srd(P.x, 2048 * 640 * 2), sws = mk_srd(P.ws, WS_END);
  const __amdgpu_buffer_rsrc_t rws = RSRC(P.ws, WS_END);
  __builtin_amdgcn_s_setprio(3);
  STDECL; STAMP(0);
  u2 ep; u2 xr[8]; u4 wr4, wi4, F16r, F16i; u32 wr1, wi1;
  {
    const u32 vo = lane < 40 ? (u32)(fw * 320 + lane * 8) : OOB;
#pragma unroll
    for (int j = 0; j < 8; ++j) asm volatile("buffer_load_dwordx2 %0, %1, %2, %3 offen" : "=v"(xr[j]) : "v"(vo), "s"(sx), "s"((u32)((a + 256 * j) * 1280)));
    u32 vz = WS_EPOCH;
    asm volatile("buffer_load_dwordx2 %0, %1, %2, 0 offen sc1" : "=v"(ep) : "v"(vz), "s"(sws));
    const u4 stab = mk_srd(g_tab, 20480);
    asm volatile("buffer_load_dwordx4 %0, %1, %2, 0 offen" : "=v"(F16r) : "v"(lane * 16), "s"(stab));
    asm volatile("buffer_load_dwordx4 %0, %1, %2, 0 offen offset:1024" : "=v"(F16i) : "v"(lane * 16), "s"(stab));
#pragma unroll
    for (int n = 0; n < 5; ++n) { const int i = min(fw * 5 + n, 17); glds<0>(lane * 16, stab, 2048 + i * 1024, 0, lds_base() + (i < 8 ? L_TABA + i * 1024 : L_TABB + (i - 8) * 1024), 0); }
  }
  __builtin_amdgcn_s_barrier();
  const int k1 = (xl == 0) ? 0 : (xl == 1) ? 4 : (xl >> 1);
  const bool mir = (xl >= 2) && (xl & 1);
  __builtin_amdgcn_s_setprio(0);
  STAMP(1);
  f4 tc[3], ts[3], bc, bs;
  {
    const float sc = fw == 0 ? 0.5f : 1.0f;
#pragma unroll
    for (int t = 0; t < 3; ++t) {
#pragma unroll
      for (int r = 0; r < 4; ++r) {
        float rev = (float)((16 * t + 4 * g + r) * l15 * 16 + a * fw * 5) * (1.0f / 10240.0f);
        tc[t][r] = __builtin_amdgcn_cosf(rev) * sc; ts[t][r] = -__builtin_amdgcn_sinf(rev) * sc;
      }
      PIN("+v"(tc[t]), "+v"(ts[t]));
    }
  }
  const float t4c = __builtin_amdgcn_cosf((float)a * (1.0f / 512.0f)), t4s = -__builtin_amdgcn_sinf((float)a * (1.0f / 512.0f));
  u32 tA = pk_h2(t4s, -t4c), tB = pk_h2(t4c, t4s);
  const u32 zp = L_R1 + fw * 2560, op = fw == 0 ? L_R2 + 1024 : zp;
  u32 zb, zr, tabp = lane * 16, redp = L_R2 + (lane & 7) * 4, o0 = op + l15 * 4 + g * 256, o1, o2, tso, orp = op + ((lane >> 2) * 10 + (lane & 3) * 3) * 4;
  {
    const int dd = 160 * fw + 4 * min(lane, 39), d1 = dd / 40, d2 = dd - d1 * 40;
    zb = L_R1 + d2 * 64 + ((d1 + (d2 & 12)) & 15) * 4;
    zr = zp + l15 * 64 + ((4 * g + (l15 & 12)) & 15) * 4;
    const u32 om = op + 3856 - 192 - l15 * 4 - g * 256;
    o1 = (fw == 0 && g > 0) ? om - 1024 : o0 + 1024;
    o2 = g >= 2 ? L_R2 + 8192 + lane * 4 : fw == 0 ? om - 2048 : o0 + 2048;
    tso = (mask & 1) ? WS_T + (2 * fw * 256 + a) * 2048 + lane * 16 : OOB;
    PIN("+v"(zb), "+v"(zr), "+v"(tabp), "+v"(redp), "+v"(o0), "+v"(orp), "+v"(tA), "+v"(tB));
    PIN("+v"(o1), "+v"(o2), "+v"(tso));
  }
  u32 fs = 0;
#define FSYNC() do { arrive(C_F); fs += 4; lwait(C_F, fs); } while (0)
  asm volatile("s_waitcnt vmcnt(8)" : "+v"(xr[0]), "+v"(xr[1]), "+v"(xr[2]), "+v"(xr[3]), "+v"(xr[4]), "+v"(xr[5]), "+v"(xr[6]), "+v"(xr[7]) :: "memory");
  __builtin_amdgcn_s_setprio(FPA);
  STAMP(2);
  {
    float v[16], w[8], y[4];
#pragma unroll
    for (int j = 0; j < 8; ++j) {
      v[j] = dot2b(xr[j].y, 0x3f803f80u, dot2b(xr[j].x, 0x3f803f80u, 0.f));
      v[8 + j] = dot2b(xr[j].y, xr[j].y, dot2b(xr[j].x, xr[j].x, 0.f));
    }
#pragma unroll
    for (int i = 0; i < 8; ++i) { auto r = __builtin_amdgcn_permlane32_swap(__float_as_uint(v[i]), __float_as_uint(v[i + 8]), false, false); w[i] = __uint_as_float(r[0]) + __uint_as_float(r[1]); }
#pragma unroll
    for (int i = 0; i < 4; ++i) { auto r = __builtin_amdgcn_permlane16_swap(__float_as_uint(w[i]), __float_as_uint(w[i + 4]), false, false); y[i] = __uint_as_float(r[0]) + __uint_as_float(r[1]); }
#pragma unroll
    for (int i = 0; i < 4; ++i) y[i] = dpp_ror_add<8>(y[i]);
#pragma unroll
    for (int i = 0; i < 4; ++i) y[i] = dpp_ror_add<4>(y[i]);
#pragma unroll
    for (int i = 0; i < 4; ++i) y[i] = dpp_ror_add<2>(y[i]);
#pragma unroll
    for (int i = 0; i < 4; ++i) y[i] = dpp_ror_add<1>(y[i]);
    *lp<f4>(L_R2 + fw * 64 + g * 16) = f4{y[0], y[1], y[2], y[3]};
  }
  asm volatile("s_waitcnt vmcnt(0)" : "+v"(ep), "+v"(F16r), "+v"(F16i) :: "memory");
  if (fw == 0) { *lp<u64>(C_TAG) = (((u64)ep.y << 32) | ep.x) + 1; arrive(C_TAGV); }
  arrive(C_F); fs += 4;
  f2 d[8][2];
#pragma unroll
  for (int j = 0; j < 8; ++j) { d[j][0] = f2{bflo(xr[j].x), bfhi(xr[j].x)}; d[j][1] = f2{bflo(xr[j].y), bfhi(xr[j].y)}; PIN("+v"(d[j][0]), "+v"(d[j][1])); }
  lwait(C_F, fs);
  STAMP(3);
  u4 F40[3][3][2];
  u64 tag;
  {
    const u2 tg = *lp<u2>(C_TAG);
    float s1 = 0, s2 = 0;
#pragma unroll
    for (int w = 0; w < 4; ++w) { s1 += *lp<float>(redp + w * 64); s2 += *lp<float>(redp + w * 64 + 32); }
    tag = ((u64)__builtin_amdgcn_readfirstlane(tg.y) << 32) | (u32)__builtin_amdgcn_readfirstlane(tg.x);
    const float md = s1 * (1.0f / 640.0f);
    const float var = fmaxf(s2 * (1.0f / 640.0f) - md * md, 0.f);
    const float mb = rbf(md), den = rbf(__builtin_amdgcn_sqrtf(rbf(rbf(var) + 1e-5f)));
    const float rd = __builtin_amdgcn_rcpf(den), nm = -mb * rd;
    {
      u32 pl = (__float_as_uint(mb) >> 16) | __float_as_uint(den);
      st16_sc1(u4{pl, (u32)tag, pl, ~(u32)tag}, (ft < 8 && (mask & 1)) ? WS_STATS + (a * 8 + lane) * 16 : OOB, sws);
    }
#pragma unroll
    for (int q = 0; q < 8; ++q) {
      float rq = __uint_as_float(__builtin_amdgcn_readlane(__float_as_uint(rd), q)), nq = __uint_as_float(__builtin_amdgcn_readlane(__float_as_uint(nm), q));
      d[q][0] = d[q][0] * rq + nq; d[q][1] = d[q][1] * rq + nq;
    }
    u32 zo[2][2][4];
#pragma unroll
    for (int h = 0; h < 2; ++h) {
      f2 a0 = d[0][h] + d[4][h], a1 = d[1][h] + d[5][h], a2 = d[2][h] + d[6][h], a3 = d[3][h] + d[7][h];
      f2 b0 = d[0][h] - d[4][h], b1 = d[1][h] - d[5][h], b2 = d[2][h] - d[6][h], b3 = d[3][h] - d[7][h];
      f2 e = a0 + a2, o = a1 + a3, u0 = e + o, u4_ = e - o, u2r = a0 - a2, u2i = a3 - a1;
      f2 m = b1 - b3, p = b1 + b3; const float r2 = 0.70710678118654752f;
      f2 u1r = b0 + m * r2, u3r = b0 - m * r2, u1i = -b2 - p * r2, u3i = b2 - p * r2;
#pragma unroll
      for (int e2 = 0; e2 < 2; ++e2) { zo[h][e2][0] = pk_h2(u0[e2], u4_[e2]); zo[h][e2][1] = pk_h2(u1r[e2], u1i[e2]); zo[h][e2][2] = pk_h2(u2r[e2], u2i[e2]); zo[h][e2][3] = pk_h2(u3r[e2], u3i[e2]); }
    }
    if (lane < 40) {
#pragma unroll
      for (int i = 0; i < 4; ++i)
#pragma unroll
        for (int p = 0; p < 4; ++p) *lp<u32>(zb + i * 64 + p * 2560) = zo[i >> 1][i & 1][p];
    }
  }
#pragma unroll
  for (int i = 0; i < 18; ++i) F40[i / 6][(i / 2) % 3][i & 1] = *lp<u4>(tabp + (i < 8 ? L_TABA + i * 1024 : L_TABB + (i - 8) * 1024));
  FSYNC();
  STAMP(4);
  {
    u4 dat[3];
#pragma unroll
    for (int t = 0; t < 3; ++t) {
      u4 A = *lp<u4>(zr + t * 1024);
      if (t == 2 && l15 >= 8) A = u4{0, 0, 0, 0};
      f4 re = mfh(A, F16r, f4{0, 0, 0, 0}), im = mfh(A, F16i, f4{0, 0, 0, 0});
      dat[t] = twid_pack(re, im, tc[t], ts[t]);
    }
    asm volatile("" ::: "memory");
#pragma unroll
    for (int u = 0; u < 3; ++u) {
      f4 re = {0, 0, 0, 0}, im = {0, 0, 0, 0};
#pragma unroll
      for (int t = 0; t < 3; ++t) { re = mfh(F40[u][t][0], dat[t], re); im = mfh(F40[u][t][1], dat[t], im); }
      u4 o = pack4(re, im);
      if (u > 0 && fw == 0 && (u == 2 || g > 0)) o = u4{o.w, o.z, o.y, o.x};
#pragma unroll
      for (int r = 0; r < 4; ++r) *lp<u32>((u == 0 ? o0 : u == 1 ? o1 : o2) + r * 64) = o[r];
    }
    asm volatile("" ::: "memory");
    if (fw != 0) {
      u4 v[4];
#pragma unroll
      for (int n = 0; n < 4; ++n) v[n] = u4{*lp<u32>(orp + n * 640), *lp<u32>(orp + n * 640 + 4), *lp<u32>(orp + n * 640 + 8), (u32)tag};
#pragma unroll
      for (int n = 0; n < 4; ++n) st16_sc1s(v[n], tso, sws, (n & 1) * 1024 + (n >> 1) * 256 * 2048);
    } else {
      u32 z[2][3], m[2][3];
#pragma unroll
      for (int n = 0; n < 2; ++n)
#pragma unroll
        for (int e = 0; e < 3; ++e) { z[n][e] = *lp<u32>(orp + n * 640 + e * 4); m[n][e] = *lp<u32>(orp + n * 640 + e * 4 + 1296); }
      if (lane == 0) m[0][0] = z[0][0];
      if (lane == 63) z[1][1] = m[1][1];
#pragma unroll
      for (int n = 0; n < 2; ++n) {
        u4 p0, p4;
#pragma unroll
        for (int e = 0; e < 3; ++e) {
          h2 zz = ash2(z[n][e]), mc = ash2(m[n][e] ^ 0x80000000u);
          h2 s = zz + mc, df = zz - mc;
          h2 q = h2{df.x, df.x} * ash2(tA) + h2{df.y, df.y} * ash2(tB);
          p0[e] = asu(s); p4[e] = asu(q);
        }
        p0.w = (u32)tag; p4.w = (u32)tag;
        st16_sc1s(p0, tso, sws, n * 1024); st16_sc1s(p4, tso, sws, n * 1024 + 256 * 2048);
      }
    }
  }
  fs += 4; arrive(C_F);
  __builtin_amdgcn_s_setprio(0);
  STAMP(5);
  SB(); PIN("+v"(lane)); g = lane >> 4; l15 = lane & 15; ft = fw * 64 + lane;
  const bool extra = (xl < 2) && (il == 31);
  if (fw == 3) {
    if ((mask & 8) && il < 28) {
      const u32 so = WS_STATS + ((xl * 32 + (il >= 14 ? 16 : 0)) * 8 + lane) * 16, co = WS_CS + ((il % 14) * 128 + lane) * 16;
      const bool half = (il % 14) == 13;
      const u32 b1v = __builtin_amdgcn_raw_buffer_load_b32(RSRC(P.b1, 1728 * 2), (il % 14) * 256 + lane * 4, 0, 0);
      u4 v[4];
      _Pragma("nounroll") for (int sp = 0; sp < SPIN_MAX; ++sp) {
        v[0] = __builtin_amdgcn_raw_buffer_load_b128(rws, so, 0, 16); v[1] = __builtin_amdgcn_raw_buffer_load_b128(rws, so + 1024, 0, 16);
        v[2] = __builtin_amdgcn_raw_buffer_load_b128(rws, co, 0, 16); v[3] = __builtin_amdgcn_raw_buffer_load_b128(rws, half ? co : co + 1024, 0, 16);
        u32 bad = 0;
#pragma unroll
        for (int i = 0; i < 4; ++i) bad |= (v[i].y ^ (u32)tag) | (v[i].w ^ ~(u32)tag) | (v[i].x ^ v[i].z);
        if (__all(bad == 0)) break;
        __builtin_amdgcn_s_sleep(4);
      }
#pragma unroll
      for (int i = 0; i < 2; ++i) {
        const float rd = __builtin_amdgcn_rcpf(bfhi(v[i].x));
        *lp<f2>(L_STAT + ((lane & 7) * 16 + (lane >> 3) + 8 * i) * 8) = f2{rd, -bflo(v[i].x) * rd};
        *lp<u32>(L_CS + (lane + 64 * i) * 4) = v[2 + i].x;
      }
      *lp<u32>(L_B1 + lane * 4) = b1v;
      arrive(C_STAT);
    }
  }
  {
    const int row = mir ? (8 - k1) + 8 * (255 - ft) : k1 + 8 * ft;
    const int col0 = mir ? 311 - 10 * il : 10 * il;
    u32 vo = (u32)(row * 321 + col0) * 2;
    asm volatile("global_load_dwordx4 %0, %1, %2" : "=v"(wr4) : "v"(vo), "s"(P.wr));
    asm volatile("global_load_dword %0, %1, %2 offset:16" : "=v"(wr1) : "v"(vo), "s"(P.wr));
    asm volatile("global_load_dwordx4 %0, %1, %2" : "=v"(wi4) : "v"(vo), "s"(P.wi));
    asm volatile("global_load_dword %0, %1, %2 offset:16" : "=v"(wi1) : "v"(vo), "s"(P.wi));
  }
  WAITV(0);
  PIN("+v"(wr4), "+v"(wr1), "+v"(wi4), "+v"(wi1) :: "memory");
  lwait(C_F, fs);
  STAMP(6);
  {
    const int q2 = 4 * fw + g;
    const u32 wb = L_R2 + l15 * 64 + ((q2 + 4 * (l15 >> 1)) & 15) * 4;
    const u32 cj = mir ? 0x80000000u : 0u;
    u32 rr[5] = {wr4.x, wr4.y, wr4.z, wr4.w, wr1}, ii[5] = {wi4.x, wi4.y, wi4.z, wi4.w, wi1};
    u32 el[10];
#pragma unroll
    for (int e = 0; e < 10; ++e) el[e] = __builtin_amdgcn_perm(ii[e >> 1], rr[e >> 1], (e & 1) ? 0x07060302u : 0x05040100u) ^ cj;
    const int q = k1 + 8 * ft, mq = (2048 - q) & 2047;
    if (il == 0) {
      const int ro = mir ? q : mq, co = mir ? 320 : 0;
      float orr = bflo(P.wr[ro * 321 + co]), oi = bflo(P.wi[ro * 321 + co]); if (!mir) oi = -oi;
      u32 mn = mir ? el[9] : el[0];
      mn = pk_b2((bflo(mn) + orr) * 0.5f, (bfhi(mn) + oi) * 0.5f);
      if (mir) el[9] = mn; else el[0] = mn;
    }
#pragma unroll
    for (int e = 0; e < 10; ++e) *lp<u32>(wb + (mir ? 9 - e : e) * 1024) = el[e];
    if (extra) *lp<u32>(wb + 10240) = pk_b2((bflo(P.wr[q * 321 + 320]) + bflo(P.wr[mq * 321 + 320])) * 0.5f, (bflo(P.wi[q * 321 + 320]) - bflo(P.wi[mq * 321 + 320])) * 0.5f);
  }
  u32 go, gi, br, wbr, yso;
  {
#pragma unroll
    for (int r = 0; r < 4; ++r) { float rev = (float)((4 * g + r) * l15) * (-1.0f / 256.0f); bc[r] = __builtin_amdgcn_cosf(rev); bs[r] = __builtin_amdgcn_sinf(rev); }
    go = WS_T + (xl * 256 + fw * 64 + (lane >> 2)) * 2048 + il * 64 + (lane & 3) * 16;
    gi = L_TABA + (fw * 64 + (lane >> 2)) * 64 + (((lane & 3) ^ g) * 16);
    br = L_TABA + (64 * g + l15) * 64 + ((fw ^ (l15 >> 2)) * 16);
    wbr = L_R2 + l15 * 64 + ((4 * g + 4 * (l15 >> 1)) & 15) * 4 + 3 * fw * 1024;
    yso = WS_Y + a * 16384 + fw * 4096 + lane * 16;
    PIN("+v"(bc), "+v"(bs), "+v"(go), "+v"(gi), "+v"(br), "+v"(wbr), "+v"(yso));
  }
  STAMP(7);
  __builtin_amdgcn_s_setprio(FPB);
  if (mask & 2) {
    u4 v[4];
    gather4(rws, go, 16 * 2048, (u32)tag, mask & 32, v);
    STAMP(8);
#pragma unroll
    for (int n = 0; n < 4; ++n) *lp<u4>(gi + n * 1024) = v[n];
    FSYNC();
    STAMP(9);
#pragma unroll
    for (int i = 0; i < 4; ++i) v[i] = *lp<u4>(br + i * 1024);
    u4 O[3];
#pragma unroll
    for (int sl = 0; sl < 3; ++sl) {
      const u4 A = {v[0][sl], v[1][sl], v[2][sl], v[3][sl]};
      const u4 Wv = *lp<u4>(wbr + sl * 1024);
      f4 z = {0, 0, 0, 0};
      u4 D = twid_pack(mfh(A, F16r, z), mfh(A, F16i, z), bc, bs);
      f4 xr_ = mfh(F16r, D, z), xi_ = mfh(F16i, D, z);
      f4 wre = {bflo(Wv.x), bflo(Wv.y), bflo(Wv.z), bflo(Wv.w)}, wim = {bfhi(Wv.x), bfhi(Wv.y), bfhi(Wv.z), bfhi(Wv.w)};
      u4 Pp = pack4(xr_ * wre - xi_ * wim, -(xr_ * wim) - xi_ * wre);
      u4 D3 = twid_pack(mfh(Pp, F16r, z), mfh(Pp, F16i, z), bc, bs);
      O[sl] = pack4(mfh(F16r, D3, z), mfh(F16i, D3, z));
    }
#pragma unroll
    for (int i = 0; i < 4; ++i) *lp<u4>(br + i * 1024) = u4{O[0][i], O[1][i], O[2][i], (u32)tag};
    FSYNC();
#pragma unroll
    for (int n = 0; n < 4; ++n) v[n] = *lp<u4>(gi + n * 1024);
#pragma unroll
    for (int n = 0; n < 4; ++n) st16_sc1s(v[n], yso, sws, n * 1024);
  }
  fs += 4; arrive(C_F);
  __builtin_amdgcn_s_setprio(0);
  if (a == 0 && fw == 0 && (mask & 32)) st8_sc1_lane0(tag, WS_EPOCH, sws);
  STAMP(10);
  STAMP(11);
  SB(); PIN("+v"(lane)); g = lane >> 4; l15 = lane & 15; ft = fw * 64 + lane;
  u32 cr, ra[3][4], vw, vw2, vr = L_R2 + (40 * fw + min(lane, 39)) * 16, sso;
  {
    const float sc = fw == 0 ? 0.125f : 0.25f;
#pragma unroll
    for (int t = 0; t < 3; ++t) {
#pragma unroll
      for (int r = 0; r < 4; ++r) {
        float rev = (float)((16 * t + l15) * (4 * g + r) * 16 + a * fw * 5) * (1.0f / 10240.0f);
        tc[t][r] = __builtin_amdgcn_cosf(rev) * sc; ts[t][r] = -__builtin_amdgcn_sinf(rev) * sc;
      }
      PIN("+v"(tc[t]), "+v"(ts[t]));
    }
    go = WS_Y + (fw * 64 + (lane >> 2)) * 16384 + a * 64 + (lane & 3) * 16;
    gi = L_R1 + ((fw * 64 + (lane >> 2)) * 10 + (lane & 3) * 3) * 4;
    br = (lane & 3) < 3 ? gi + 4 : L_TABA + fw * 2048 + (lane >> 2) * 8;
    cr = L_R1 + fw * 2560 + l15 * 4 + g * 256;
#pragma unroll
    for (int s = 0; s < 3; ++s)
#pragma unroll
      for (int i = 0; i < 4; ++i) {
        const int k = l15 + 16 * min(16 * s + 4 * g + i, 39), m = k <= 320 ? k : 640 - k;
        ra[s][i] = (L_R1 + m * 4) | (k <= 320 ? 0u : 0x80000000u);
      }
    vw = L_R2 + fw * 2560 + (160 * g + l15) * 4;
    vw2 = l15 < 8 ? vw + 128 : L_R2 + 10240 + lane * 4;
    sso = lane < 40 ? WS_S + a * 10240 + (40 * fw + lane) * 16 : OOB;
    PIN("+v"(go), "+v"(gi), "+v"(br), "+v"(cr), "+v"(vw), "+v"(vw2), "+v"(vr), "+v"(sso));
#pragma unroll
    for (int s = 0; s < 3; ++s) PIN("+v"(ra[s][0]), "+v"(ra[s][1]), "+v"(ra[s][2]), "+v"(ra[s][3]));
  }
  lwait(C_F, fs);
  __builtin_amdgcn_s_setprio(FPC);
  STAMP(12);
  if (mask & 4) {
    u4 v[4];
    gather4(rws, go, 16 * 16384, (u32)tag, mask & 32, v);
#pragma unroll
    for (int n = 0; n < 4; ++n) { *lp<u32>(gi + n * 640) = v[n].x; *lp<u32>(br + n * 640) = v[n].y; *lp<u32>(br + n * 640 + 4) = v[n].z; }
    FSYNC();
    STAMP(13);
    u4 dat[3];
    if (fw != 0) {
#pragma unroll
      for (int s = 0; s < 3; ++s)
#pragma unroll
        for (int i = 0; i < 4; ++i) {
          u32 v = *lp<u32>(cr + s * 1024 + i * 64);
          dat[s][i] = (s == 2 && g >= 2) ? 0u : v;
        }
    } else {
      const u32 e0 = *lp<u32>(L_TABA + 120 + 640), e4 = *lp<u32>(L_TABA + 120 + 1920);
#pragma unroll
      for (int s = 0; s < 3; ++s)
#pragma unroll
        for (int i = 0; i < 4; ++i) {
          const u32 cj = ra[s][i] & 0x80000000u, ad = ra[s][i] & 0x7fffffffu;
          u32 y0 = *lp<u32>(ad), y4 = *lp<u32>(ad + 1280);
          if (s == 1 && i == 0) { if (lane == 16) { y0 = e0; y4 = e4; } }
          h2 q = ash2(y4);
          h2 v4 = h2{q.x, q.x} * ash2(tB) - h2{q.y, q.y} * ash2(tA);
          h2 vv = ash2(asu(v4) ^ cj), yy = ash2(y0 ^ cj);
          h2 zc = yy + h2{vv.y, -vv.x};
          dat[s][i] = (s == 2 && g >= 2) ? 0u : asu(zc);
        }
    }
#pragma unroll
    for (int t = 0; t < 3; ++t) {
      f4 re = {0, 0, 0, 0}, im = {0, 0, 0, 0}, z = {0, 0, 0, 0};
#pragma unroll
      for (int s = 0; s < 3; ++s) { re = mfh(dat[s], F40[t][s][0], re); im = mfh(dat[s], F40[t][s][1], im); }
      u4 D = twid_pack(re, im, tc[t], ts[t]);
      u4 O = pack4(mfh(F16r, D, z), mfh(F16i, D, z));
#pragma unroll
      for (int r = 0; r < 4; ++r) *lp<u32>((t < 2 ? vw + 64 * t : vw2) + r * 160) = O[r];
    }
    FSYNC();
    STAMP(14);
    u4 v0 = *lp<u4>(vr), v1 = *lp<u4>(vr + 2560), v2 = *lp<u4>(vr + 5120), v3 = *lp<u4>(vr + 7680);
    float s[8][4];
#pragma unroll
    for (int e = 0; e < 4; ++e) {
      h2 q0 = ash2(v0[e]), q1 = ash2(v1[e]), q2 = ash2(v2[e]), q3 = ash2(v3[e]);
      float e0 = (float)q0.x - (float)q0.y, e1 = (float)q0.x + (float)q0.y;
      float Pp = (float)q1.x + (float)q3.x, M = (float)q1.x - (float)q3.x, Q = (float)q1.y + (float)q3.y, N = (float)q1.y - (float)q3.y;
      float A0 = e0 + (float)q2.x, A2 = e0 - (float)q2.x, A1 = e1 + (float)q2.y, A3 = e1 - (float)q2.y;
      const float r2 = 0.70710678118654752f; float B1 = M + Q, B3 = Q - M;
      s[0][e] = A0 + Pp; s[4][e] = A0 - Pp; s[2][e] = A2 + N; s[6][e] = A2 - N;
      s[1][e] = A1 + B1 * r2; s[5][e] = A1 - B1 * r2; s[3][e] = A3 + B3 * r2; s[7][e] = A3 - B3 * r2;
    }
#pragma unroll
    for (int jp = 0; jp < 4; ++jp) {
      u4 o = {pk_b2(s[2 * jp][0], s[2 * jp][1]), pk_b2(s[2 * jp][2], s[2 * jp][3]), pk_b2(s[2 * jp + 1][0], s[2 * jp + 1][1]), pk_b2(s[2 * jp + 1][2], s[2 * jp + 1][3])};
      st16_sc1(o, (sso + jp * 2560), sws);
    }
  }
  STAMP(15);
  WAITV(0);
  if (mask & 4) arrive_pub<1>(C_F, fs + 3, tag, WS_FS + a * 128, sws);
  STAMP(16);
  STDUMP(8 + fw);
}

constexpr u32 wslot(int n) { return 65536u + (n % 3) * 20480u; }
constexpr u32 hslot(int n) { return (n & 3) * 16384u; }
DEV u64 get_tag() { lwait(C_TAGV, 1); u64 t = *lp<u64>(C_TAG); return ((u64)__builtin_amdgcn_readfirstlane((u32)(t >> 32)) << 32) | (u32)__builtin_amdgcn_readfirstlane((u32)t); }
DEV f2 gelu2(f2 v) {
  const float c1 = -2.302208198f, c2 = -0.1029432397f;
  f2 e = v * (v * v * c2 + c1);
  f2 t = f2{__builtin_amdgcn_exp2f(e.x), __builtin_amdgcn_exp2f(e.y)} + 1.0f;
  return v * f2{__builtin_amdgcn_rcpf(t.x), __builtin_amdgcn_rcpf(t.y)};
}

DEV u32 peek(u32 addr) { return *(volatile LAS u32*)(uintptr_t)addr; }
DEV void need4(u32 seen, u32 addr, u32 target) {
  asm volatile("v_cmp_gt_u32 vcc, %2, %0\n\ts_nop 1\n\ts_cbranch_vccz 2f\n1:\n\tds_read_b32 %0, %1\n\ts_waitcnt lgkmcnt(0)\n\tv_cmp_gt_u32 vcc, %2, %0\n\ts_nop 1\n\t"
               "s_cbranch_vccnz 1b\n2:" : "+v"(seen) : "v"(addr), "s"(target) : "memory", "vcc");
}

DEV void g_path(const Params& P, int w, int lane, int xl, int il) {
  const int g = lane >> 4, l15 = lane & 15, gt = w * 64 + lane;
  const int wm = w >> 1, wn = w & 1;
  const u32 mask = KMASK, lb = lds_base();
  const u4 sws = mk_srd(P.ws, WS_END);
  const u32 fulla = lb + C_FULL + (lane & 3) * 4;
  u32 gs = 0;
  STDECL; STAMP(0);
  if (w == 3) *lp<u32>(L_CNT + lane * 4) = 0;
  asm volatile("s_waitcnt lgkmcnt(0)" ::: "memory");
  __builtin_amdgcn_s_barrier();
  if (!(mask & 8) || il >= 28) {
    const int un = xl * 4 + il - 28;
    if ((mask & 8) && un < 27) {
      const u4 sw1 = mk_srd(P.w1, 640 * 1728 * 2);
      const u32 vo = (160 * w + (lane >> 3)) * 3456 + un * 128 + (lane & 7) * 16;
      const u32 one = (l15 & 1) ? 0x3f800000u : 0x3f80u;
      const u4 sel = {l15 >> 1 == 0 ? one : 0u, l15 >> 1 == 1 ? one : 0u, l15 >> 1 == 2 ? one : 0u, l15 >> 1 == 3 ? one : 0u};
      u4 v[20];
#pragma unroll
      for (int i = 0; i < 20; ++i) asm volatile("buffer_load_dwordx4 %0, %1, %2, %3 offen" : "=v"(v[i]) : "v"(vo), "s"(sw1), "s"(i * 8 * 3456));
      asm volatile("s_waitcnt vmcnt(0)" : "+v"(v[0]), "+v"(v[1]), "+v"(v[2]), "+v"(v[3]), "+v"(v[4]), "+v"(v[5]), "+v"(v[6]), "+v"(v[7]), "+v"(v[8]), "+v"(v[9]) :: "memory");
      PIN("+v"(v[10]), "+v"(v[11]), "+v"(v[12]), "+v"(v[13]), "+v"(v[14]), "+v"(v[15]), "+v"(v[16]), "+v"(v[17]), "+v"(v[18]), "+v"(v[19]));
      f4 s = {0, 0, 0, 0};
#pragma unroll
      for (int i = 0; i < 20; ++i) s = mfb(sel, v[i], s);

#pragma unroll
      for (int r = 0; r < 4; ++r) s[r] = dpp_ror_add<8>(s[r]);
      if (g < 2 && l15 < 8) *lp<f4>(w * 256 + l15 * 32 + g * 16) = s;
      arrive(C_G); lwait(C_G, 4); gs = 4;
      const u64 tag = get_tag();
      const u32 t = __float_as_uint((*lp<float>(lane * 4) + *lp<float>(256 + lane * 4)) + (*lp<float>(512 + lane * 4) + *lp<float>(768 + lane * 4)));
      st16_sc1(u4{t, (u32)tag, t, ~(u32)tag}, w == 0 ? WS_CS + (un * 64 + lane) * 16 : OOB, sws);
      asm volatile("s_waitcnt lgkmcnt(0)" ::: "memory");
    }
    post(C_FREE, w, 10); arrive(C_P1);
  } else {
    const int pm = il >= 14, ct = il - 14 * pm, f0 = ct * 128;
    const int phi = ((l15 >> 3) & 1) | ((g & 1) << 1);
    u32 ad[6];
#pragma unroll
    for (int t = 0; t < 4; ++t) ad[t] = 16384 + wn * 4096 + g * 1024 + (l15 >> 2) * 128 + ((lane >> 1) & 1) * 16 + 8 * (lane & 1) + ((t ^ phi) * 32);
    ad[4] = wm * 8192 + l15 * 128 + ((g ^ (l15 >> 1)) * 16); ad[5] = ad[4] ^ 64;
    f4 acc[4][4];
#pragma unroll
    for (int m = 0; m < 4; ++m)
#pragma unroll
      for (int t = 0; t < 4; ++t) acc[m][t] = f4{0, 0, 0, 0};
    u4 X[2][4]; u2 A[2][4][2];
#define RD1(e, i) do { if ((i) < 4) X[e][i] = *lp<u4>(ad[4 + (e)] + (i) * 2048); else A[e][((i) - 4) >> 1][(i) & 1] = trh(ad[((i) - 4) >> 1] + (e) * 8192 + ((i) & 1) * 512); } while (0)
#define STEP1(e, NXT, FR) do { _Pragma("unroll") for (int t = 0; t < 4; ++t) { \
        const u4 Af = {A[e][t][0].x, A[e][t][0].y, A[e][t][1].x, A[e][t][1].y}; \
        _Pragma("unroll") for (int m = 0; m < 4; ++m) { \
          acc[m][t] = mfb(Af, X[e][m], acc[m][t]); SB(); \
          if (m < 3) { if (NXT) RD1(1 - (e), 3 * t + m); } \
          else if ((e) == 1 && t == 0) { asm volatile("s_waitcnt lgkmcnt(%0)" :: "i"((NXT) ? 3 : 0) : "memory"); post(C_FREE, w, FR); } \
          SB(); } } \
        PIN("+v"(acc[0][0]), "+v"(acc[0][1]), "+v"(acc[0][2]), "+v"(acc[0][3]), "+v"(acc[1][0]), "+v"(acc[1][1]), "+v"(acc[1][2]), "+v"(acc[1][3])); \
        PIN("+v"(acc[2][0]), "+v"(acc[2][1]), "+v"(acc[2][2]), "+v"(acc[2][3]), "+v"(acc[3][0]), "+v"(acc[3][1]), "+v"(acc[3][2]), "+v"(acc[3][3])); \
        SB(); } while (0)
    PIN("+v"(acc[0][0]), "+v"(acc[0][1]), "+v"(acc[0][2]), "+v"(acc[0][3]), "+v"(acc[1][0]), "+v"(acc[1][1]), "+v"(acc[1][2]), "+v"(acc[1][3]));
    PIN("+v"(acc[2][0]), "+v"(acc[2][1]), "+v"(acc[2][2]), "+v"(acc[2][3]), "+v"(acc[3][0]), "+v"(acc[3][1]), "+v"(acc[3][2]), "+v"(acc[3][3]));
    PIN("+v"(ad[0]), "+v"(ad[1]), "+v"(ad[2]), "+v"(ad[3]), "+v"(ad[4]), "+v"(ad[5]));
    wait4<0>(fulla, 1);
    STAMP(2);
#pragma unroll
    for (int i = 0; i < 12; ++i) RD1(0, i);
    SB();
    _Pragma("nounroll") for (int n = 0; n < 9; ++n) {
      const u32 seen = peek(fulla);
      SB();
      STEP1(0, true, 0);
      need4(seen, fulla, n + 2);
      const u32 dl = (n & 3) == 3 ? (u32)-98304 : 32768u;
#pragma unroll
      for (int t = 0; t < 6; ++t) ad[t] += dl;
      SB();
      STEP1(1, true, n + 1);
    }
    STEP1(0, true, 0);
    STEP1(1, false, 10);
    STAMP(7);
    const u64 tag = get_tag();
    lwait(C_STAT, 1);
    f2 st[4];
#pragma unroll
    for (int m = 0; m < 4; ++m) st[m] = *lp<f2>(L_STAT + (64 * wm + 16 * m + l15) * 8);
    STAMP(8);
#pragma unroll
    for (int pp = 0; pp < 2; ++pp) {
      f4 cv[2], bv[2];
#pragma unroll
      for (int h = 0; h < 2; ++h) {
        const int fo = 64 * wn + 16 * (2 * pp + h) + 4 * g;
        cv[h] = *lp<f4>(L_CS + fo * 4);
        const u2 b = *lp<u2>(L_B1 + fo * 2);
        bv[h] = f4{bflo(b.x), bfhi(b.x), bflo(b.y), bfhi(b.y)};
      }
      const int p = (f0 + 64 * wn) / 32 + pp;
      const u32 ho = p < 54 ? WS_H + ((16 * xl + 8 * pm + 4 * wm) * 54 + p) * 1024 + lane * 16 : OOB;
#pragma unroll
      for (int m = 0; m < 4; ++m) {
        u4 o;
#pragma unroll
        for (int h = 0; h < 2; ++h) {
          f4 v = acc[m][2 * pp + h] * st[m].x + (cv[h] * st[m].y + bv[h]);
          f2 lo = gelu2(f2{v.x, v.y}), hi = gelu2(f2{v.z, v.w});
          o[2 * h] = pk_b2(lo.x, lo.y); o[2 * h + 1] = pk_b2(hi.x, hi.y);
        }
        st16_sc1(o, ho + m * 55296, sws);
      }
    }
    STAMP(11);
    WAITV(0); STAMP(12);
    arrive_pub<1>(C_P1, 3, tag, WS_FH + ((xl * 2 + pm) * 16 + ct) * 128, sws);
  }
  if (!(mask & 16)) { STDUMP(w); return; }
  {
    const __amdgpu_buffer_rsrc_t rws = RSRC(P.ws, WS_END);
    const u4 sx = mk_srd(P.x, 2048 * 640 * 2);
    const int rt = il >> 3, nt = il & 7, n0 = 80 * nt, a0 = 32 * xl + 16 * (rt >> 1);
    u32 xo[3], so[3], ro[3]; u2 xv[3][2], bv[3];
    {
      const u4 sb2 = mk_srd(P.b2, 640 * 2);
#pragma unroll
      for (int n = 0; n < 3; ++n) {
        const u32 it = min(gt + 256 * n, 639), pa = (it * 52429u) >> 20, c4 = it - pa * 20, pp = pa >> 4, ao = pa & 15;
        const bool ok = gt + 256 * n < 640;
        xo[n] = ok ? ((a0 + ao + 256 * (4 * (rt & 1) + 2 * pp)) * 640 + n0 + 4 * c4) * 2 : OOB;
        so[n] = ok ? WS_S + (a0 + ao) * 10240 + (2 * (rt & 1) + pp) * 2560 + ((n0 >> 2) + c4) * 16 : OOB;
        ro[n] = 86016 + (32 * pp + ao) * 336 + c4 * 16;
        asm volatile("buffer_load_dwordx2 %0, %1, %2, 0 offen" : "=v"(xv[n][0]) : "v"(xo[n]), "s"(sx));
        asm volatile("buffer_load_dwordx2 %0, %1, %2, %3 offen" : "=v"(xv[n][1]) : "v"(xo[n]), "s"(sx), "s"(327680u));
        asm volatile("buffer_load_dwordx2 %0, %1, %2, 0 offen" : "=v"(bv[n]) : "v"((n0 + 4 * c4) * 2), "s"(sb2));
      }
      PIN("+v"(so[0]), "+v"(so[1]), "+v"(so[2]), "+v"(ro[0]), "+v"(ro[1]), "+v"(ro[2]));
    }
    f4 acc[5][2];
#pragma unroll
    for (int t = 0; t < 5; ++t) { acc[t][0] = f4{0, 0, 0, 0}; acc[t][1] = acc[t][0]; }
    u4 B[3][5], H[3][2]; u32 seen = 0;
    u32 wa = 65536 + lane * 16 + wn * 5120, ha = lane * 16 + wn * 4096 + wm * 2048;
    PIN("+v"(wa), "+v"(ha));
#define RD2(q, i) do { if ((i) < 2) H[(q) % 3][i] = *lp<u4>(ha + hslot((q) >> 1) + ((q) & 1) * 8192 + (i) * 1024); \
      else B[(q) % 3][(i) - 2] = *lp<u4>(wa + (wslot((q) >> 1) - 65536) + ((q) & 1) * 10240 + ((i) - 2) * 1024); } while (0)
    STAMP(13);
    if (w == 0) {
      lwait_slow(C_P1, 4);
      if ((mask & 8) && il >= 28) lwait_slow(C_F, 24);
      poll_one<HSLP>(rws, WS_FH + ((xl * 2 + (rt >> 1)) * 16 + min(lane, 13)) * 128, get_tag());
      arrive(C_HOK);
    }
    wait4<1>(fulla, 11);
    STAMP(14);
#pragma unroll
    for (int i = 0; i < 14; ++i) RD2(i / 7, i % 7);
    seen = peek(fulla);
    SB();
#pragma unroll
    for (int q = 0; q < 27; ++q) {
      if (!(q & 1) && q + 2 < 27) ACC(4, need4(seen, fulla, 12 + (q >> 1)));
      SB();
#pragma unroll
      for (int t = 0; t < 5; ++t)
#pragma unroll
        for (int m = 0; m < 2; ++m) {
          acc[t][m] = mfb(B[q % 3][t], H[q % 3][m], acc[t][m]); SB();
          if (q + 2 < 27 && 2 * t + m < 7) RD2(q + 2, 2 * t + m);
          SB();
        }
      PIN("+v"(acc[0][0]), "+v"(acc[1][0]), "+v"(acc[2][0]), "+v"(acc[3][0]), "+v"(acc[4][0]), "+v"(acc[0][1]), "+v"(acc[1][1]), "+v"(acc[2][1]), "+v"(acc[3][1]), "+v"(acc[4][1]));
      if (!(q & 1) && q + 2 < 27) { asm volatile("s_waitcnt lgkmcnt(7)" ::: "memory"); post(C_FREE, w, 11 + (q >> 1)); }
      if (q & 1) seen = peek(fulla);
      SB();
    }
    STAMP(15);
    lwait_slow(C_SOK, 1);
    u4 sv[3];
#pragma unroll
    for (int n = 0; n < 3; ++n) asm volatile("buffer_load_dwordx4 %0, %1, %2, 0 offen sc1" : "=v"(sv[n]) : "v"(so[n]), "s"(sws));
    gs += 4; arrive(C_G); lwait(C_G, gs);
    {
      const u32 pw = 86016 + wn * 21504 + (32 * wm + l15) * 336 + g * 16;
#pragma unroll
      for (int m = 0; m < 2; ++m)
#pragma unroll
        for (int t = 0; t < 5; ++t) *lp<f4>(pw + m * 16 * 336 + t * 64) = acc[t][m];
    }
    gs += 4; arrive(C_G);
    STAMP(16);
    asm volatile("s_waitcnt vmcnt(0)" : "+v"(xv[0][0]), "+v"(xv[0][1]), "+v"(xv[1][0]), "+v"(xv[1][1]), "+v"(xv[2][0]), "+v"(xv[2][1]), "+v"(bv[0]), "+v"(bv[1]), "+v"(bv[2]),
                 "+v"(sv[0]), "+v"(sv[1]), "+v"(sv[2]) :: "memory");
    STAMP(17);
    lwait(C_G, gs);
    const __amdgpu_buffer_rsrc_t ro_ = RSRC(P.out, 2048 * 640 * 2);
#pragma unroll
    for (int n = 0; n < 3; ++n)
#pragma unroll
      for (int e = 0; e < 2; ++e) {
        f4 v = f4{bflo(bv[n].x), bfhi(bv[n].x), bflo(bv[n].y), bfhi(bv[n].y)} + *lp<f4>(ro[n] + e * 16 * 336) + *lp<f4>(ro[n] + 21504 + e * 16 * 336);
        f4 ex = v * -1.4426950408889634f, o;
#pragma unroll
        for (int r = 0; r < 4; ++r) o[r] = v[r] * __builtin_amdgcn_rcpf(1.0f + __builtin_amdgcn_exp2f(ex[r]));
        const u32 s0 = sv[n][2 * e], s1 = sv[n][2 * e + 1];
        o = o * f4{bflo(s0), bfhi(s0), bflo(s1), bfhi(s1)} + f4{bflo(xv[n][e].x), bfhi(xv[n][e].x), bflo(xv[n][e].y), bfhi(xv[n][e].y)};
        __builtin_amdgcn_raw_buffer_store_b64(u2{pk_b2(o.x, o.y), pk_b2(o.z, o.w)}, ro_, xo[n] + e * 327680, 0, 0);
      }
    STAMP(18);
    STDUMP(w);
  }
}
#define THR() WAITV(5)
DEV void l_path(const Params& P, int l, int lane, int xl, int il) {
  const int g = lane >> 4, l3 = lane >> 3;
  const u32 mask = KMASK, lb = lds_base();
  const u4 sws = mk_srd(P.ws, WS_END);
  const u32 freea = lb + C_FREE + (lane & 3) * 4;
  STDECL; STAMP(0);
  const bool gemm1 = (mask & 8) && il < 28;
  if (gemm1) {
    const int pm = il >= 14, ct = il - 14 * pm;
    const u4 sx = mk_srd(P.x, 2048 * 640 * 2), sw1 = mk_srd(P.w1, 640 * 1728 * 2);
    const u32 bvo = (8 * l + l3) * 3456 + (((lane & 7) ^ (2 * (((l3 >> 1) & 1) | ((l & 1) << 1)))) * 16);
    const u32 xvo0 = l3 * 1280 + (((lane & 7) ^ g) * 16), xvo1 = (8 + l3) * 1280 + (((lane & 7) ^ (4 + g)) * 16);
    const u32 xs0 = (32 * xl + 16 * pm + 512 * l) * 1280, bs0 = ct * 256;
    int kc = (ct + 5 * pm + xl) % 10;
    auto issue = [&](int n) {
      const u32 xs = xs0 + kc * 128, bs = bs0 + kc * 64 * 3456, lx = lb + (n & 3) * 32768 + l * 4096, lw = lb + (n & 3) * 32768 + 16384 + l * 1024;
      glds<0>(xvo0, sx, xs, 0, lx, 0); glds<0>(xvo1, sx, xs, 0, lx, 1024);
      glds<0>(bvo, sw1, bs, 0, lw, 0); glds<0>(bvo, sw1, bs, 128, lw, 4096);
      glds<0>(xvo0, sx, xs, 256 * 1280, lx, 2048); glds<0>(xvo1, sx, xs, 256 * 1280, lx, 3072);
      glds<0>(bvo, sw1, bs, 32 * 3456, lw, 8192); glds<0>(bvo, sw1, bs, 32 * 3456 + 128, lw, 12288);
      kc = kc == 9 ? 0 : kc + 1;
    };
    issue(0);
    __builtin_amdgcn_s_barrier();
    issue(1);
    STAMP(1);
    WAITV(8); post(C_FULL, l, 1); STAMP(2);
    issue(2);
    WAITV(8); post(C_FULL, l, 2);
    issue(3);
    _Pragma("nounroll") for (int n = 0; n < 6; ++n) { ACC(10, wait4<LSLP>(freea, n + 1)); ACC(11, WAITV(8)); post(C_FULL, l, n + 3); ACC(12, issue(n)); }
    arrive(C_LD);
    STAMP(3);
    WAITV(8); post(C_FULL, l, 9);
    WAITV(0); post(C_FULL, l, 10);
    STAMP(4);
  } else if (mask & 8) {
    const u4 sw2 = mk_srd(P.w2, 1728 * 640 * 2);
    const int tw = (xl * 4 + il - 28) * 4 + l;
    const u32 reg = 4096 + l * 20480;
    const u32 vo = l3 * 1280 + (((lane & 7) ^ (l3 & 6)) * 16);
    _Pragma("nounroll") for (int e = 0; e < 5; ++e) {
      const int bid = min(tw + 128 * e, 539), p = bid / 10, nb = bid - p * 10;
#pragma unroll
      for (int q = 0; q < 4; ++q) glds<0>(vo, sw2, p * 32 * 1280 + nb * 128, q * 8 * 1280, lb + reg + e * 4096, q * 1024);
      if (e == 0) __builtin_amdgcn_s_barrier();
    }
    arrive(C_LD);
    WAITV(0);
    const int row = 4 * g + ((lane & 15) >> 2);
    _Pragma("nounroll") for (int e = 0; e < 5; ++e) {
      const int bid = tw + 128 * e, p = bid / 10, nb = bid - p * 10;
      if (bid < 540)
#pragma unroll
        for (int t = 0; t < 4; ++t) {
          const u32 ad = reg + e * 4096 + row * 128 + (((2 * t + ((lane >> 1) & 1)) ^ (row & 6)) * 16) + 8 * (lane & 1);
          u2 lo = trh(ad), hi = trh(ad + 2048);
          st16_sc1(u4{lo.x, lo.y, hi.x, hi.y}, WS_W2T + ((4 * nb + t) * 54 + p) * 1024 + lane * 16, sws);
        }
    }
    STAMP(2);
    WAITV(0);
    arrive_pub<1>(C_P0, 3, get_tag(), WS_FW + (xl * 4 + il - 28) * 128, sws);
    lwait(C_P0, 4);
    STAMP(4);
  } else { __builtin_amdgcn_s_barrier(); arrive(C_LD); }
  if (!(mask & 16)) { STDUMP(4 + l); return; }
  PIN("+s"(l));
  const __amdgpu_buffer_rsrc_t rws = RSRC(P.ws, WS_END);
  const int rt = il >> 3, nt = il & 7;
  const u32 wvo = WS_W2T + 5 * nt * 55296 + lane * 16;
  u32 pk[14];
#pragma unroll
  for (int n = 0; n < 14; ++n) pk[n] = (4 * n + l + ROT2) % 54;
  const u32 hvo = WS_H + (16 * xl + 4 * rt) * 55296 + lane * 16;
  const u32 dead = l >= 2 ? OOB : 0u, lw = lb + l * 5120;
  const u32 lh = lb + l * 4096, wwr = l * 5120 + lane * 16;
  u4 R[7][5];
#define LW(n) do { _Pragma("unroll") for (int t = 0; t < 5; ++t) glds<1>((n) == 13 ? (wvo | dead) : wvo, sws, pk[n] * 1024, t * 55296, lw, wslot(n) + t * 1024); } while (0)
#define LR(n) do { _Pragma("unroll") for (int t = 0; t < 5; ++t) \
                   asm volatile("buffer_load_dwordx4 %0, %1, %2, %3 offen sc1" : "=v"(R[(n) - 3][t]) : "v"(wvo), "s"(sws), "s"(pk[n] * 1024 + t * 55296)); } while (0)
#define LH(n) do { _Pragma("unroll") for (int m = 0; m < 4; ++m) glds<1>((n) == 13 ? (hvo | dead) : hvo, sws, pk[n] * 1024, m * 55296, lh, hslot(n) + m * 1024); } while (0)
#define PINR(n) PIN("+v"(R[(n) - 3][0]), "+v"(R[(n) - 3][1]), "+v"(R[(n) - 3][2]), "+v"(R[(n) - 3][3]), "+v"(R[(n) - 3][4]) :: "memory")
  if (l == 0) { poll_one<6>(rws, WS_FW + (lane & 31) * 128, get_tag()); arrive(C_WOK); } else lwait_slow(C_WOK, 1);
  __builtin_amdgcn_s_setprio(0);
  STAMP(5);
#pragma unroll
  for (int n = 3; n < 6; ++n) { LR(n); THR(); }
  wait4<1>(freea, 7); LW(0); THR();
  wait4<1>(freea, 8); LW(1); THR(); LW(2); THR();
#pragma unroll
  for (int n = 6; n < 10; ++n) { LR(n); THR(); }
  STAMP(6);
  {
    lwait_slow(C_HOK, 1);
  }
  WAITV(0);
  wait4<0>(freea, 10);
  __builtin_amdgcn_s_setprio(2);
  STAMP(7);
  LH(0); LH(1); LH(2); LH(3);
  WAITV(12); post(C_FULL, l, 11); STAMP(8);
  WAITV(8); post(C_FULL, l, 12);
  u32 seen = 0;
#pragma unroll
  for (int n = 0; n < 12; ++n) {
    const int yo = n <= 7 ? 4 : (n + 3 < 14 ? 4 : 0);
    ACC(13, asm volatile("s_waitcnt vmcnt(%0)" :: "i"(yo) : "memory"));
    post(C_FULL, l, 13 + n);
    if (n + 3 < 14) {
      ACC(14, need4(seen, freea, 11 + n));
      if (n + 3 >= 10) LW(n + 3);
      else {
        PINR(n + 3);
#pragma unroll
        for (int t = 0; t < 5; ++t) *lp<u4>(wwr + wslot(n + 3) + t * 1024) = R[n][t];
      }
      if (n + 4 < 14) {
#pragma unroll
        for (int m = 0; m < 4; ++m) {
          if (m == 3) seen = peek(freea);
          glds<1>(n + 4 == 13 ? (hvo | dead) : hvo, sws, pk[n + 4] * 1024, m * 55296, lh, hslot(n + 4) + m * 1024);
        }
      }
    }
  }
  STAMP(9);
  if (l == 0) { poll_one<2>(rws, WS_FS + (32 * xl + 16 * (rt >> 1) + (lane & 15)) * 128, get_tag()); arrive(C_SOK); }
  STDUMP(4 + l);
}

#if defined(__HIP_DEVICE_COMPILE__) && !defined(USE_PK)
__attribute__((target("no-packed-fp32-ops")))
#endif
__global__ __launch_bounds__(768, 3) void fused_kernel(Params P) {
  const int wave = __builtin_amdgcn_readfirstlane(threadIdx.x >> 6), lane = threadIdx.x & 63;
  const int xl = blockIdx.x & 7, il = blockIdx.x >> 3;
  if (wave >= 8) { f_path(P, wave - 8, lane, xl, il); }
  else if (wave >= 4) { __builtin_amdgcn_s_setprio(LPRIO); l_path(P, wave - 4, lane, xl, il); }
  else { __builtin_amdgcn_s_setprio(GPRIO); g_path(P, wave, lane, xl, il); }
}

extern "C" void kernel_launch_mask(void* const* d_in, void* d_out, void* d_ws, unsigned mask, hipStream_t stream) {
  Params P;
  P.x = (const u16*)d_in[0]; P.wr = (const u16*)d_in[1]; P.wi = (const u16*)d_in[2]; P.w1 = (const u16*)d_in[4]; P.b1 = (const u16*)d_in[5];
  P.w2 = (const u16*)d_in[6]; P.b2 = (const u16*)d_in[7]; P.out = (u16*)d_out; P.ws = (char*)d_ws; P.mask = mask; P.pad = 0;
  fused_kernel<<<256, 768, 0, stream>>>(P);
}
extern "C" void kernel_launch(void* const* d_in, const int* in_sizes, int n_in, void* d_out, int out_size, void* d_ws, size_t ws_size, hipStream_t stream) {
  kernel_launch_mask(d_in, d_out, d_ws, 63u, stream);
}
```
